```python
import torch

def _softmax(x, axis=-1):
    return torch.softmax(x, dim=axis)
def _gelu(x):
    return torch.nn.functional.gelu(x, approximate="tanh")
def _silu(x):
    return torch.nn.functional.silu(x)
def _sigmoid(x):
    return torch.sigmoid(x)
def _layernorm(x, axis=-1, eps=1e-5):
    m = x.mean(dim=axis, keepdim=True)
    v = x.var(dim=axis, keepdim=True, unbiased=False)
    return (x - m) / torch.sqrt(v + eps)
def _scatter_add(tgt, idx, src, axis=0):
    return tgt.clone().index_add_(axis, idx, src)
def _segment_sum(src, seg, n):
    out = torch.zeros((n,) + src.shape[1:], dtype=src.dtype)
    return out.index_add_(0, seg, src)
def _pad(x, pw):
    flat = tuple(v for pair in reversed(pw) for v in pair)
    return torch.nn.functional.pad(x, flat)

kernel_name = 'gated_mix__sliding_local_dilated__sliding_block__prompt'

def setup_inputs():

    _w = (lambda a: (lambda t: t.to(torch.bfloat16) if t.is_floating_point() else t)(torch.as_tensor(a)))
    def _A_setup():
        B, S, D = 1, 2048, 512
        return {
            "x": _w((lambda *s: torch.randn(*s, dtype=torch.bfloat16) * 0.05)(B, S, D)),
            "wqkv": _w((lambda *s: torch.randn(*s, dtype=torch.bfloat16) * 1.6)(D, 3 * D)),
            "wo": _w((lambda *s: torch.randn(*s, dtype=torch.bfloat16) * 0.05)(D, D)),
        }
    _A_in = _A_setup()

    def _B_setup():
        B, S, D = 1, 2048, 512
        return {
            "x": _w((lambda *s: torch.randn(*s, dtype=torch.bfloat16) * 0.05)(B, S, D)),
            "wqkv": _w((lambda *s: torch.randn(*s, dtype=torch.bfloat16) * 1.6)(D, 3 * D)),
            "wo": _w((lambda *s: torch.randn(*s, dtype=torch.bfloat16) * 0.05)(D, D)),
        }
    _B_in = _B_setup()

    return {
        "A_x": _A_in["x"],
        "A_wqkv": _A_in["wqkv"],
        "A_wo": _A_in["wo"],
        "B_x": _B_in["x"],
        "B_wqkv": _B_in["wqkv"],
        "B_wo": _B_in["wo"],
        "gate_w": _w((lambda *s: torch.randn(*s, dtype=torch.bfloat16) * 0.05)(512) * 16.0),
    }

def reference(A_x, A_wqkv, A_wo, B_x, B_wqkv, B_wo, gate_w):
    def _A_ref(x, wqkv, wo):
        B, S, D = x.shape
        H = 32; Dh = D // H
        W, dil = 8, 2
        qkv = (lambda x, *d: x.permute(*d))((x @ wqkv).reshape(B, S, 3, H, Dh), 2, 0, 3, 1, 4)
        q, k, v = qkv[0], qkv[1], qkv[2]
        scale = Dh ** -0.5
        pos = torch.arange(S)
        off = torch.arange(W) * dil
        idx = pos[:, None] - off[None, :]
        mask = idx >= 0
        idx_c = torch.where(mask, idx, 0).reshape(-1)
        kg = k[:, :, idx_c, :].reshape(B, H, S, W, Dh)
        vg = v[:, :, idx_c, :].reshape(B, H, S, W, Dh)
        logits = torch.einsum("bhsd,bhswd->bhsw", q, kg) * scale
        logits = torch.where(mask[None, None, :, :], logits, -1e9)
        attn = _softmax(logits, axis=-1)
        o = torch.einsum("bhsw,bhswd->bhsd", attn, vg)
        return (lambda x, *d: x.permute(*d))(o, 0, 2, 1, 3).reshape(B, S, D) @ wo

    def _B_ref(x, wqkv, wo):
        B, S, D = x.shape
        H = 32; Dh = D // H; W = 256
        qkv = (lambda x, *d: x.permute(*d))((x @ wqkv).reshape(B, S, 3, H, Dh), 2, 0, 3, 1, 4)
        q, k, v = qkv[0], qkv[1], qkv[2]
        scores = torch.einsum("bhqd,bhkd->bhqk", q, k) * (Dh ** -0.5)
        idx = torch.arange(S)
        mask = (idx[:, None] - idx[None, :] >= 0) & (idx[:, None] - idx[None, :] < W)
        scores = torch.where(mask[None, None], scores, -1e9)
        p = _softmax(scores, axis=-1)
        o = torch.einsum("bhqk,bhkd->bhqd", p, v)
        return (lambda x, *d: x.permute(*d))(o, 0, 2, 1, 3).reshape(B, S, D) @ wo

    x = A_x
    g = _sigmoid((x * gate_w).sum(-1, keepdims=True))
    a = _A_ref(x, A_wqkv, A_wo)
    b = _B_ref(x, B_wqkv, B_wo)
    return g * a + (1 - g) * b
```

```cpp
#include <hip/hip_runtime.h>
#include <hip/hip_bf16.h>
typedef unsigned short u16;
__device__ __forceinline__ float bf2f(u16 v){ return __uint_as_float(((unsigned)v)<<16); }
__device__ __forceinline__ u16 f2bf(float f){ unsigned u=__float_as_uint(f); u += 0x7fffu + ((u>>16)&1u); return (u16)(u>>16); }
constexpr int S=2048,D=512,H=32,DH=16;

__global__ void k_qkv(const u16* x,const u16* wa,const u16* wb,u16* qkv){
  int n = blockIdx.x*64 + (threadIdx.x&63); int t = blockIdx.y*4 + (threadIdx.x>>6);
  const u16* w = n<1536? wa+n : wb+(n-1536);
  float acc=0; for(int k=0;k<D;++k) acc += bf2f(x[t*D+k])*bf2f(w[k*1536]);
  qkv[t*3072+n]=f2bf(acc);
}
__global__ void k_attn(const u16* qkv,const u16* x,const u16* gw,u16* o){
  int idx = blockIdx.x*blockDim.x+threadIdx.x; int h=idx&31; int br=(idx>>5)&1; int t=idx>>6;
  const u16* base = qkv + br*1536;
  float q[16]; for(int d=0;d<16;++d) q[d]=bf2f(base[t*3072+h*16+d]);
  int W = br?256:8, dil = br?1:2;
  float m=-1e30f;
  for(int w=0;w<W;++w){ int j=t-w*dil; if(j<0)break; float s=0; for(int d=0;d<16;++d) s+=q[d]*bf2f(base[j*3072+512+h*16+d]); s*=0.25f; m=fmaxf(m,s);}
  float l=0, acc[16]; for(int d=0;d<16;++d) acc[d]=0;
  for(int w=0;w<W;++w){ int j=t-w*dil; if(j<0)break; float s=0; for(int d=0;d<16;++d) s+=q[d]*bf2f(base[j*3072+512+h*16+d]); s*=0.25f; float p=__expf(s-m); l+=p; for(int d=0;d<16;++d) acc[d]+=p*bf2f(base[j*3072+1024+h*16+d]);}
  for(int d=0;d<16;++d) o[t*1024+br*512+h*16+d]=f2bf(acc[d]/l);
}
__global__ void k_out(const u16* o,const u16* woa,const u16* wob,const u16* x,const u16* gw,u16* out){
  int n = blockIdx.x*64 + (threadIdx.x&63); int t = blockIdx.y*4 + (threadIdx.x>>6);
  float g=0; for(int k=0;k<D;++k) g += bf2f(x[t*D+k])*bf2f(gw[k]); g = 1.f/(1.f+__expf(-g));
  float a=0,b=0; for(int k=0;k<D;++k){ a += bf2f(o[t*1024+k])*bf2f(woa[k*512+n]); b += bf2f(o[t*1024+512+k])*bf2f(wob[k*512+n]); }
  out[t*D+n]=f2bf(g*a+(1.f-g)*b);
}
extern "C" void kernel_launch(void* const* d_in, const int* in_sizes, int n_in, void* d_out, int out_size, void* d_ws, size_t ws_size, hipStream_t stream){
  const u16* x=(const u16*)d_in[0]; const u16* wa=(const u16*)d_in[1]; const u16* woa=(const u16*)d_in[2];
  const u16* wb=(const u16*)d_in[4]; const u16* wob=(const u16*)d_in[5]; const u16* gw=(const u16*)d_in[6];
  u16* qkv=(u16*)d_ws; u16* o=qkv+S*3072;
  k_qkv<<<dim3(48,512),256,0,stream>>>(x,wa,wb,qkv);
  k_attn<<<S*64/256,256,0,stream>>>(qkv,x,gw,o);
  k_out<<<dim3(8,512),256,0,stream>>>(o,woa,wob,x,gw,(u16*)d_out);
}
```

```cpp
#include <hip/hip_runtime.h>
#include <hip/hip_bf16.h>
typedef unsigned short u16;
__device__ __forceinline__ float bf2f(u16 v){ return __uint_as_float(((unsigned)v)<<16); }
__device__ __forceinline__ u16 f2bf(float f){ unsigned u=__float_as_uint(f); u += 0x7fffu + ((u>>16)&1u); return (u16)(u>>16); }
constexpr int S=2048,D=512,H=32,DH=16;

__global__ void k_qkv(const u16* x,const u16* wa,const u16* wb,u16* qkv){
  int n = blockIdx.x*64 + (threadIdx.x&63); int t = blockIdx.y*4 + (threadIdx.x>>6);
  const u16* w = n<1536? wa+n : wb+(n-1536);
  float acc=0; for(int k=0;k<D;++k) acc += bf2f(x[t*D+k])*bf2f(w[k*1536]);
  qkv[t*3072+n]=f2bf(acc);
}
__global__ void k_attn(const u16* qkv,const u16* x,const u16* gw,u16* o){
  int idx = blockIdx.x*blockDim.x+threadIdx.x; int h=idx&31; int br=(idx>>5)&1; int t=idx>>6;
  const u16* base = qkv + br*1536;
  float q[16]; for(int d=0;d<16;++d) q[d]=bf2f(base[t*3072+h*16+d]);
  int W = br?256:8, dil = br?1:2;
  float m=-1e30f;
  for(int w=0;w<W;++w){ int j=t-w*dil; if(j<0)break; float s=0; for(int d=0;d<16;++d) s+=q[d]*bf2f(base[j*3072+512+h*16+d]); s*=0.25f; m=fmaxf(m,s);}
  float l=0, acc[16]; for(int d=0;d<16;++d) acc[d]=0;
  for(int w=0;w<W;++w){ int j=t-w*dil; if(j<0)break; float s=0; for(int d=0;d<16;++d) s+=q[d]*bf2f(base[j*3072+512+h*16+d]); s*=0.25f; float p=__expf(s-m); l+=p; for(int d=0;d<16;++d) acc[d]+=p*bf2f(base[j*3072+1024+h*16+d]);}
  for(int d=0;d<16;++d) o[t*1024+br*512+h*16+d]=f2bf(acc[d]/l);
}
__global__ void k_out(const u16* o,const u16* woa,const u16* wob,const u16* x,const u16* gw,u16* out){
  int n = blockIdx.x*64 + (threadIdx.x&63); int t = blockIdx.y*4 + (threadIdx.x>>6);
  float g=0; for(int k=0;k<D;++k) g += bf2f(x[t*D+k])*bf2f(gw[k]); g = 1.f/(1.f+__expf(-g));
  float a=0,b=0; for(int k=0;k<D;++k){ a += bf2f(o[t*1024+k])*bf2f(woa[k*512+n]); b += bf2f(o[t*1024+512+k])*bf2f(wob[k*512+n]); }
  out[t*D+n]=f2bf(g*a+(1.f-g)*b);
}

typedef unsigned v4u __attribute__((ext_vector_type(4)));
__device__ __forceinline__ void glds16(const void* g, unsigned ldsaddr){
  unsigned keep;
  asm volatile("s_mov_b32 %0, m0\n\ts_mov_b32 m0, %2\n\ts_nop 0\n\tglobal_load_lds_dwordx4 %1, off\n\ts_mov_b32 m0, %0" : "=&s"(keep) : "v"(g), "s"(ldsaddr) : "memory");
}
template<int N> __device__ __forceinline__ void waitvm(){ asm volatile("s_waitcnt vmcnt(%0)"::"i"(N):"memory"); }
__global__ __launch_bounds__(512) void k_probe(const u16* __restrict__ x,const u16* __restrict__ wa,const u16* __restrict__ wb){
  __shared__ __attribute__((aligned(16))) char smem[163840];
  unsigned long long r0=__builtin_amdgcn_s_memrealtime();
  int b=blockIdx.x; int xcd=b&7, r=b>>3;
  int lane=threadIdx.x&63; int w=__builtin_amdgcn_readfirstlane(threadIdx.x>>6);
  unsigned base=(unsigned)(size_t)smem;
  int tb=(xcd&1)*8+(r&7), hg=(xcd>>1)*4+(r>>3);
  constexpr int PPW=5;
  #pragma unroll 1
  for(int kc=0;kc<8;++kc){
    #pragma unroll
    for(int i=0;i<PPW;++i){
      int p=w*PPW+i; const u16* g;
      if(p<16){ int row=tb*128+p*8+(lane>>3); g=x+row*512+kc*64+(lane&7)*8; }
      else { int q=p-16; int seg=q*8+(lane>>3); int kr=seg/3, s=seg%3; g=((hg&1)?wb:wa)+(kc*64+kr)*1536+s*512+(hg>>1)*64+(lane&7)*8; }
      glds16(g,base+(unsigned)((kc&3)*40960+p*1024));
    }
    waitvm<3*PPW>();
  }
  waitvm<0>();
  __syncthreads();
  unsigned long long r1=__builtin_amdgcn_s_memrealtime();
  if (b==100) { unsigned long long tgt=r0+(r1-r0)*1000ull; if (tgt>r0+2000000ull) tgt=r0+2000000ull; while(__builtin_amdgcn_s_memrealtime()<tgt) __builtin_amdgcn_s_sleep(32); }
}
extern "C" void kernel_launch(void* const* d_in, const int* in_sizes, int n_in, void* d_out, int out_size, void* d_ws, size_t ws_size, hipStream_t stream){
  const u16* x=(const u16*)d_in[0]; const u16* wa=(const u16*)d_in[1]; const u16* woa=(const u16*)d_in[2];
  const u16* wb=(const u16*)d_in[4]; const u16* wob=(const u16*)d_in[5]; const u16* gw=(const u16*)d_in[6];
  u16* qkv=(u16*)d_ws; u16* o=qkv+S*3072;
  k_probe<<<256,512,0,stream>>>(x,wa,wb);
  k_qkv<<<dim3(48,512),256,0,stream>>>(x,wa,wb,qkv);
  k_attn<<<S*64/256,256,0,stream>>>(qkv,x,gw,o);
  k_out<<<dim3(8,512),256,0,stream>>>(o,woa,wob,x,gw,(u16*)d_out);
}
```

```cpp
#include <hip/hip_runtime.h>
#include <hip/hip_bf16.h>

typedef unsigned short u16;
typedef unsigned u32;
typedef unsigned long long u64;
typedef short bf16x8 __attribute__((ext_vector_type(8)));
typedef short s16x4 __attribute__((ext_vector_type(4)));
typedef __bf16 bf16x2 __attribute__((ext_vector_type(2)));
typedef float f32x2 __attribute__((ext_vector_type(2)));
typedef float f32x4 __attribute__((ext_vector_type(4)));
typedef float f32x16 __attribute__((ext_vector_type(16)));
typedef unsigned u32x2 __attribute__((ext_vector_type(2)));
typedef unsigned u32x4 __attribute__((ext_vector_type(4)));
typedef __amdgpu_buffer_rsrc_t rsrc_t;
#define LAS __attribute__((address_space(3)))
#define DI __device__ __forceinline__
#define SBAR() __builtin_amdgcn_sched_barrier(0)

#define STAMPS 0

constexpr u32 WS_F = 0;
constexpr u32 WS_E = 65536;
constexpr u32 WS_KV = 131072;
constexpr u32 WS_O = WS_KV + 64 * 32 * 3072;
constexpr u32 WS_END = WS_O + 64 * 2 * 32 * 1024;
constexpr u32 WS_ST = WS_END;
constexpr u32 OOB = 0x7F000000u;
constexpr u64 TAGC = 0x9E3779B97F4A7C15ull;
constexpr u32 L_W = 0;
constexpr u32 L_X = 131072;
constexpr u32 L_GW = 155648;
constexpr u32 L_GP = 156672;
constexpr u32 L_CNT = 158720;
constexpr u32 L_END = 158736;

template <int N> struct IC { static constexpr int v = N; };
template <int I, int N, class F> DI void sfor(F&& f) { if constexpr (I < N) { f(IC<I>{}); sfor<I + 1, N>(f); } }

DI rsrc_t mkrs(const void* p, u32 bytes) { return __builtin_amdgcn_make_buffer_rsrc((void*)p, (short)0, (int)bytes, 0x00020000); }
DI void dma(u32 lds, u32 voff, rsrc_t rs, u32 soff) {
  asm volatile("s_mov_b32 m0, %0\n\ts_nop 0\n\tbuffer_load_dwordx4 %1, %2, %3 offen lds" ::"s"(lds), "v"(voff), "s"(rs), "s"(soff) : "m0", "memory");
}
template <int N> DI void wait_bar() { asm volatile("s_waitcnt vmcnt(%0) lgkmcnt(0)\n\ts_barrier" ::"i"(N) : "memory"); }
DI u32x4 ld16_sc1(u32 voff, rsrc_t rs, u32 soff) { u32x4 r; asm volatile("buffer_load_dwordx4 %0, %1, %2, %3 offen sc1" : "=v"(r) : "v"(voff), "s"(rs), "s"(soff) : "memory"); return r; }
DI u32x2 ld8_sc1(u32 voff, rsrc_t rs, u32 soff) { u32x2 r; asm volatile("buffer_load_dwordx2 %0, %1, %2, %3 offen sc1" : "=v"(r) : "v"(voff), "s"(rs), "s"(soff) : "memory"); return r; }
DI void st16_sc1(u32x4 v, u32 voff, rsrc_t rs, u32 soff) { asm volatile("buffer_store_dwordx4 %0, %1, %2, %3 offen sc1\n\ts_nop 1" ::"v"(v), "v"(voff), "s"(rs), "s"(soff) : "memory"); }
DI void st8_sc1(u32x2 v, u32 voff, rsrc_t rs, u32 soff) { asm volatile("buffer_store_dwordx2 %0, %1, %2, %3 offen sc1" ::"v"(v), "v"(voff), "s"(rs), "s"(soff) : "memory"); }
DI void pin4(u32x4& a) { asm volatile("" : "+v"(a)); }
DI void pin(u32x2& a, u32x2& b) { asm volatile("" : "+v"(a), "+v"(b)); }
template <int N> DI void wait1(u32x4& a) { asm volatile("s_waitcnt vmcnt(%1)" : "+v"(a) : "i"(N)); }
template <int N> DI void wait3(u32x4& a, u32x4& b, u32x4& c) { asm volatile("s_waitcnt vmcnt(%3)" : "+v"(a), "+v"(b), "+v"(c) : "i"(N)); }
DI void ld_kv(u32x4& k, u32x4& v0, u32x4& v1, u32 vk, u32 vv, rsrc_t rs, u32 so) {
  asm volatile("buffer_load_dwordx4 %0, %3, %5, %6 offen sc1\n\t"
               "s_mov_b64 exec, %7\n\t"
               "buffer_load_dwordx4 %1, %4, %5, %6 offen offset:1024 sc1\n\t"
               "buffer_load_dwordx4 %2, %4, %5, %6 offen offset:1536 sc1\n\t"
               "s_mov_b64 exec, -1"
               : "=&v"(k), "+v"(v0), "+v"(v1)
               : "v"(vk), "v"(vv), "s"(rs), "s"(so), "s"(0x0000FFFF0000FFFFull)
               : "memory");
}
template <int N> DI void waitv() { asm volatile("s_waitcnt vmcnt(%0)" ::"i"(N) : "memory"); }
DI void nop3() { asm volatile("s_nop 2" ::: "memory"); }
DI void bar() { asm volatile("s_barrier" ::: "memory"); }
DI u32x2 tagof(u32x2 e) { const u64 t = (((u64)e[1] << 32) | e[0]) + TAGC; return u32x2{(u32)t, (u32)(t >> 32)}; }
DI const LAS char* opaque(const LAS char* p) { asm volatile("" : "+v"(p)); return p; }
DI u64 rtime() { u64 t; asm volatile("s_memrealtime %0\n\ts_waitcnt lgkmcnt(0)" : "=s"(t)::"memory"); return t; }
DI u32 pk2(float a, float b) { f32x2 v = {a, b}; return __builtin_bit_cast(u32, __builtin_convertvector(v, bf16x2)); }
template <int B> DI u32x4 pk8(const f32x16& a, float s) { return u32x4{pk2(a[B] * s, a[B + 1] * s), pk2(a[B + 2] * s, a[B + 3] * s), pk2(a[B + 4] * s, a[B + 5] * s), pk2(a[B + 6] * s, a[B + 7] * s)}; }
template <int B> DI u32x4 pk8(const f32x16& a) { return u32x4{pk2(a[B], a[B + 1]), pk2(a[B + 2], a[B + 3]), pk2(a[B + 4], a[B + 5]), pk2(a[B + 6], a[B + 7])}; }
DI float dot2(u32 a, u32 b, float c) { return __builtin_amdgcn_fdot2_f32_bf16(__builtin_bit_cast(bf16x2, a), __builtin_bit_cast(bf16x2, b), c, false); }
DI void swap16(u32x4& a, u32x4& b) {
#pragma unroll
  for (int i = 0; i < 4; ++i) { auto r = __builtin_amdgcn_permlane16_swap(a[i], b[i], false, false); a[i] = r[0]; b[i] = r[1]; }
}
DI f32x16 mf32(u32x4 a, u32x4 b, f32x16 c) { return __builtin_amdgcn_mfma_f32_32x32x16_bf16(__builtin_bit_cast(bf16x8, a), __builtin_bit_cast(bf16x8, b), c, 0, 0, 0); }
DI f32x4 mf16(u32x4 a, u32x4 b, f32x4 c) { return __builtin_amdgcn_mfma_f32_16x16x32_bf16(__builtin_bit_cast(bf16x8, a), __builtin_bit_cast(bf16x8, b), c, 0, 0, 0); }
DI u32x4 lds16(const LAS char* p) { return *(const LAS u32x4*)p; }
DI u32x2 tr8(const LAS char* p) { return __builtin_bit_cast(u32x2, __builtin_amdgcn_ds_read_tr16_b64_v4i16((LAS s16x4*)p)); }
DI u32x4 cat(u32x2 a, u32x2 b) { return u32x4{a[0], a[1], b[0], b[1]}; }
template <int KS> DI void gate_dot(float& acc, u32x4 xv, u32x4 gv, int hh) {
  asm volatile("s_cmp_lg_u32 %9, %10\n\ts_cbranch_scc1 1f\n\tv_dot2c_f32_bf16 %0, %1, %5\n\tv_dot2c_f32_bf16 %0, %2, %6\n\tv_dot2c_f32_bf16 %0, %3, %7\n\tv_dot2c_f32_bf16 %0, %4, %8\n1:"
               : "+v"(acc) : "v"(xv[0]), "v"(xv[1]), "v"(xv[2]), "v"(xv[3]), "v"(gv[0]), "v"(gv[1]), "v"(gv[2]), "v"(gv[3]), "s"(hh), "i"(KS) : "scc");
}
DI u32x4 ldtr(const LAS char* p, u32 second) {
  u32x2 lo = __builtin_bit_cast(u32x2, __builtin_amdgcn_ds_read_tr16_b64_v4i16((LAS s16x4*)p));
  u32x2 hi = __builtin_bit_cast(u32x2, __builtin_amdgcn_ds_read_tr16_b64_v4i16((LAS s16x4*)(p + second)));
  return u32x4{lo[0], lo[1], hi[0], hi[1]};
}
DI float xhalf_max(float v) { auto r = __builtin_amdgcn_permlane32_swap(__float_as_uint(v), __float_as_uint(v), false, false); return fmaxf(__uint_as_float(r[0]), __uint_as_float(r[1])); }
DI void poll(u32 voff, rsrc_t rs, u32x2 tag, bool need) {
#pragma unroll 1
  for (int spin = 0; spin < (1 << 20); ++spin) {
    u32x2 f;
    asm volatile("buffer_load_dwordx2 %0, %1, %2, 0 offen sc1\n\ts_waitcnt vmcnt(0)" : "=&v"(f) : "v"(voff), "s"(rs) : "memory");
    if (!__any(need && (f[0] != tag[0] || f[1] != tag[1]))) break;
    __builtin_amdgcn_s_sleep(1);
  }
}
#define STAMP(i)

DI void tile_full(u32x4 k, u32x4 q, u32x4 v0, u32x4 v1, const f32x16& c, f32x16& o) {
  f32x16 s = mf32(k, q, c);
#pragma unroll
  for (int i = 0; i < 16; ++i) s[i] = __builtin_amdgcn_exp2f(s[i]);
  o = mf32(v0, pk8<0>(s), o);
  o = mf32(v1, pk8<8>(s), o);
}
DI void tile_upper(u32x4 k, u32x4 q, u32x4 v1, const f32x16& c, f32x16& o) {
  f32x16 s = mf32(k, q, c);
#pragma unroll
  for (int i = 8; i < 16; ++i) s[i] = __builtin_amdgcn_exp2f(s[i]);
  o = mf32(v1, pk8<8>(s), o);
}
template <int B> DI float tile_max(u32x4 k, u32x4 q, const f32x16& c, float m) {
  f32x16 s = mf32(k, q, c);
#pragma unroll
  for (int i = B; i < 16; ++i) m = fmaxf(m, s[i]);
  return m;
}
DI int opaque(int v) { asm volatile("" : "+v"(v)); return v; }
template <bool FAR> DI f32x16 mask_causal(int lane) {
  const int dq = (lane & 31) - 4 * (lane >> 5);
  f32x16 c;
#pragma unroll
  for (int r = 0; r < 16; ++r) { const int jr = (r & 3) + 8 * (r >> 2); c[r] = __uint_as_float((u32)((FAR ? jr - 1 - dq : dq - jr) >> 31) & 0xFF800000u); }
  return c;
}
template <bool PREV> DI f32x16 mask_band(int lane) {
  const u64 band = (u64)0x5555u << ((lane & 31) + 18);
  const u32 n = ~((PREV ? (u32)band : (u32)(band >> 32)) >> (4 * (lane >> 5)));
  f32x16 c;
#pragma unroll
  for (int r = 0; r < 16; ++r) { const int jr = (r & 3) + 8 * (r >> 2); c[r] = (PREV && r < 8) ? 0.f : __uint_as_float((u32)((int)(n << (31 - jr)) >> 31) & 0xFF800000u); }
  return c;
}
DI f32x16 addc(const f32x16& c, float x) { f32x16 r;
#pragma unroll
  for (int i = 0; i < 16; ++i) r[i] = c[i] + x;
  return r; }
DI bool row_ok(const f32x16& o) {
  float a = fmaxf(fmaxf(fmaxf(fabsf(o[0]), fabsf(o[1])), fmaxf(fabsf(o[2]), fabsf(o[3]))), fmaxf(fmaxf(fabsf(o[4]), fabsf(o[5])), fmaxf(fabsf(o[6]), fabsf(o[7]))));
  float s = (o[0] + o[1]) + (o[2] + o[3]) + (o[4] + o[5]) + (o[6] + o[7]);
  return o[8] >= 0x1p-80f && o[8] <= 0x1p100f && a <= 0x1p120f && s == s;
}

__global__ __launch_bounds__(512, 2) void gated_mix_kernel(const u16* __restrict__ x, const u16* __restrict__ wa, const u16* __restrict__ woa,
                                                           const u16* __restrict__ wb, const u16* __restrict__ wob, const u16* __restrict__ gw,
                                                           u16* __restrict__ out, char* __restrict__ ws) {
  __shared__ __attribute__((aligned(1024))) char smem_[L_END];
  const LAS char* sm = (const LAS char*)smem_;
  const u32 smb = (u32)(size_t)sm;
  const int lane = threadIdx.x & 63;
  const int w = __builtin_amdgcn_readfirstlane(threadIdx.x >> 6);
  STAMP(0);
  asm volatile("" ::"s"(x), "s"(wa), "s"(woa), "s"(wb), "s"(wob), "s"(gw), "s"(out), "s"(ws));
  const int b = blockIdx.x, xcd = b & 7, rr = b >> 3;
  const int tb = (xcd & 1) * 16 + (rr & 15), hg = (xcd >> 1) * 2 + (rr >> 4);
  const int wg = tb * 8 + hg;
  const int rot = (rr & 7) + 4 * (rr >> 4) + 2 * ((xcd >> 1) & 1) + (xcd >> 2);
  const int tq = w & 1, hh = w >> 1, head = hg * 4 + hh, tile = tb * 2 + tq;
  const int h = lane >> 5, g = lane >> 4, i16 = lane & 15;

  const rsrc_t rX = mkrs(x, 2048 * 512 * 2), rA = mkrs(wa, 512 * 1536 * 2), rB = mkrs(wb, 512 * 1536 * 2), rW = mkrs(ws, WS_END);

  const u32 vX = (u32)((tb * 64 + 8 * w + (lane >> 3)) * 1024 + (((lane & 7) ^ ((4 * (w & 1) + g) & 7)) * 16));
  const u32 vWA = (u32)((lane >> 3) * 3072 + (((lane & 7) ^ ((g & 1) << 1)) * 16));
  const u32 vWB = (u32)((lane >> 3) * 3072 + (((lane & 7) ^ ((2 + (g & 1)) << 1)) * 16));
  const u32 sW0 = (u32)(8 * w * 3072 + hg * 128);
  auto kst = [&](int i) { return (u32)((i + rot) & 7); };
  auto issue_x = [&](auto I) { constexpr int s = I.v; if constexpr (s < 8) dma(smb + L_X + (s % 3) * 8192 + w * 1024, vX, rX, kst(s) * 128); };
  auto issue_w = [&](auto I, auto P) {
    constexpr int s = I.v, p = P.v;
    if constexpr (s < 12) {
      const u32 so = s < 8 ? sW0 + kst(s) * (64 * 3072) + 1024 + (p >> 1) * 1024 : sW0 + kst(2 * (s - 8) + (p >> 1)) * (64 * 3072);
      dma(smb + L_W + (s % 3) * 32768 + w * 1024 + p * 8192, (p & 1) ? vWB : vWA, (p & 1) ? rB : rA, so);
    }
  };
  auto issue = [&](auto I) { issue_x(I); sfor<0, 4>([&](auto P) { issue_w(I, P); }); };
  if (w == 7) dma(smb + L_GW, lane * 16, mkrs(gw, 1024), 0);
  issue(IC<0>{});
  bar();
  const u32 cons_off = lane < 24 ? (u32)(wg * 256 + lane * 8) : OOB;
  u32 prod_off = OOB;
  if (lane < 4 && tb + lane + 1 < 32) prod_off = (u32)(((tb + lane + 1) * 8 + hg) * 256 + lane * 8);
  if (lane >= 8 && lane < 24) prod_off = (u32)((tb * 8 + (lane & 7)) * 256 + ((lane & 24) + hg) * 8);
  u32x2 ctag = ld8_sc1(cons_off, rW, WS_E);
  u32x2 ptag = ld8_sc1(prod_off, rW, WS_E);
  issue(IC<1>{});
  if (threadIdx.x < 2) *(LAS u32*)(sm + L_CNT + threadIdx.x * 4) = 0;

  const int xrow = 32 * tq + (lane & 31);
  const LAS char* xa[4];
#pragma unroll
  for (int ks = 0; ks < 4; ++ks) xa[ks] = sm + L_X + xrow * 128 + (((2 * ks + h) ^ ((xrow >> 1) & 7)) * 16);
  const LAS char* wl0 = sm + L_W + (g & 1) * 8192 + (8 * h + (i16 >> 2)) * 128 + ((hh ^ (2 * (g & 1) + (i16 >> 3))) * 32) + (i16 & 3) * 8;
  const LAS char* wl2 = opaque(wl0 + 65536);
  auto wslot = [&](int s) { return s % 3 == 2 ? wl2 : wl0 + (s % 3) * 32768; };
  const LAS char* gl = sm + L_GW + h * 16 + hh * 32;

  u32x4 xf[32];
  f32x16 accK = {}, accV = {};
  float gacc = 0.f;
  u32x2 kl[4], kh_[4], vl[4], vh[4]; u32x4 gv;
  auto rX_ = [&](auto I, auto K) { constexpr int i = I.v, ks = K.v; xf[4 * i + ks] = lds16(xa[ks] + (i % 3) * 8192); };
  auto rK0 = [&](auto I, auto K) { constexpr int i = I.v, ks = K.v; kl[ks] = tr8(wslot(i) + ks * 2048); };
  auto rK1 = [&](auto I, auto K) { constexpr int i = I.v, ks = K.v; kh_[ks] = tr8(wslot(i) + ks * 2048 + 512); };
  auto rV0 = [&](auto I, auto K) { constexpr int i = I.v, ks = K.v; vl[ks] = tr8(wslot(i) + 16384 + ks * 2048); };
  auto rV1 = [&](auto I, auto K) { constexpr int i = I.v, ks = K.v; vh[ks] = tr8(wslot(i) + 16384 + ks * 2048 + 512); };
  auto mK = [&](auto I, auto K) { constexpr int i = I.v, ks = K.v; if constexpr (i >= 0) accK = mf32(cat(kl[ks], kh_[ks]), xf[4 * i + ks], accK); SBAR(); };
  auto mV = [&](auto I, auto K) { constexpr int i = I.v, ks = K.v; if constexpr (i >= 0) accV = mf32(xf[4 * i + ks], cat(vl[ks], vh[ks]), accV); SBAR(); };
  auto gd = [&](auto I, auto K) { constexpr int i = I.v, ks = K.v; if constexpr (i >= 0) gate_dot<ks>(gacc, xf[4 * i + ks], gv, hh); };
  constexpr IC<0> c0; constexpr IC<1> c1; constexpr IC<2> c2; constexpr IC<3> c3;
  sfor<0, 8>([&](auto I) {
    constexpr int i = I.v;
    constexpr IC<i - 1> P; constexpr IC<i + 2> N;
    wait_bar<(i == 0 ? 7 : i == 7 ? 4 : 5)>();
    if constexpr (i == 1) { pin(ctag, ptag); ctag = tagof(ctag); ptag = tagof(ptag); }
    if constexpr (i == 0) STAMP(1);
    mK(P, c2); rX_(I, c0); rK0(I, c0); rK1(I, c0); SBAR();
    mV(P, c2); rV0(I, c0); rV1(I, c0); rX_(I, c1); gd(P, c2); SBAR();
    issue_x(N); SBAR();
    mK(P, c3); rK0(I, c1); rK1(I, c1); rV0(I, c1); SBAR();
    mV(P, c3); rV1(I, c1); rX_(I, c2); rK0(I, c2); gd(P, c3); SBAR();
    issue_w(N, c0); SBAR();
    mK(I, c0); rK1(I, c2); rV0(I, c2); rV1(I, c2); gv = lds16(gl + kst(i) * 128); SBAR();
    issue_w(N, c1); SBAR();
    mV(I, c0); rX_(I, c3); rK0(I, c3); rK1(I, c3); SBAR();
    mK(I, c1); rV0(I, c3); rV1(I, c3); SBAR();
    issue_w(N, c2); SBAR();
    mV(I, c1); gd(I, c0); gd(I, c1); SBAR();
    issue_w(N, c3);
  });
  f32x16 accQ = {};
  u32x4 KA, KB, VA, VB, wq[8];
  const u32 kv_own = WS_KV + (u32)(tile * 32 + head) * 3072;
  const u32 vV = (u32)((g & 1) * 512 + h * 256 + i16 * 16);
  auto rd2 = [&](auto J, auto K) { constexpr int j = J.v, ks = K.v; wq[ks] = ldtr(wslot(8 + j) + (ks >> 2) * 16384 + (ks & 3) * 2048, 512); };
  auto mm2 = [&](auto J, auto K) { constexpr int j = J.v, ks = K.v; accQ = mf32(wq[ks], xf[8 * j + ks], accQ); };
  sfor<0, 4>([&](auto J) {
    constexpr int j = J.v;
    constexpr IC<10 + j> N;
    wait_bar<(j == 0 ? 4 : j == 1 ? 7 : j == 2 ? 4 : 0)>();
    STAMP(9 + j);
    if constexpr (j == 2) { if (w == 0) st8_sc1(ptag, lane < 4 ? prod_off : OOB, rW, WS_F); }
    sfor<0, 4>([&](auto K) {
      if constexpr (j == 0) { constexpr IC<7> P; if constexpr (K.v == 0) mK(P, c2); if constexpr (K.v == 1) { mV(P, c2); gd(P, c2); } if constexpr (K.v == 2) mK(P, c3); if constexpr (K.v == 3) { mV(P, c3); gd(P, c3); } }
      else mm2(IC<j - 1>{}, IC<4 + K.v>{});
      SBAR();
      rd2(J, K);
      SBAR();
      if constexpr (K.v & 1) { issue_w(N, IC<K.v / 2>{}); SBAR(); }
    });
    sfor<0, 4>([&](auto K) {
      mm2(J, K);
      SBAR();
      rd2(J, IC<4 + K.v>{});
      SBAR();
      if constexpr (j == 0 && K.v == 0) {
        nop3();
        *(LAS float*)(sm + L_GP + xrow * 32 + hh * 8 + h * 4) = gacc;
        KA = pk8<0>(accK); KB = pk8<8>(accK);
        VA = pk8<0>(accV); VB = pk8<8>(accV);
        swap16(VA, VB);
        u32x4 t0 = VA, AH = KA;
        swap16(t0, AH);
        st16_sc1(KB, lane * 16, rW, kv_own);
        st16_sc1(VB, vV, rW, kv_own + 1024);
        st16_sc1(AH, lane * 16, rW, kv_own + 2048);
        SBAR();
      }
      if constexpr (K.v & 1) { issue_w(N, IC<2 + K.v / 2>{}); SBAR(); }
    });
  });
  wait_bar<63>();
  sfor<4, 8>([&](auto K) { mm2(IC<3>{}, K); });
  STAMP(13);
  poll(cons_off + WS_F, rW, ctag, lane < 4 && tb - lane - 1 >= 0);
  STAMP(14);
  u32x4 kf[8], v0[8], v1[8];
  auto ldt = [&](auto Dd) {
    constexpr int d = Dd.v;
    const bool ok = tile - d - 1 >= 0;
    const u32 one = ok ? 0x3F803F80u : 0u;
    v0[d] = u32x4{one, one, one, one}; v1[d] = v0[d];
    ld_kv(kf[d], v0[d], v1[d], lane * 16, vV, rW, ok ? kv_own - (d + 1) * 32 * 3072 : OOB);
  };
  const rsrc_t rOA = mkrs(woa, 512 * 512 * 2), rOB = mkrs(wob, 512 * 512 * 2);
  const u32 vO = (u32)((lane >> 3) * 1024 + (((lane & 7) ^ ((g & 3) << 1)) * 16));
  auto ldwo = [&](auto P) {
    constexpr int p = P.v, sl = p >> 2, i = p & 3;
    dma(smb + L_W + sl * 32768 + (w * 4 + i) * 1024, vO, sl < 2 ? rOA : rOB, (u32)(((sl & 1) * 256 + w * 32 + i * 8) * 1024 + hg * 128));
  };
  auto signal = [&](int which) {
    u32 old = 0;
    if (lane == 0) old = __hip_atomic_fetch_add((LAS u32*)(sm + L_CNT + which * 4), 1u, __ATOMIC_RELAXED, __HIP_MEMORY_SCOPE_WORKGROUP);
    if (__builtin_amdgcn_readfirstlane(old) == 7) st8_sc1(ptag, (lane >> 3) == 1 + which ? prod_off : OOB, rW, WS_F);
  };
  u32x4 AH = ld16_sc1(lane * 16, rW, tile >= 1 ? kv_own - 32 * 3072 + 2048 : OOB);
  ldt(IC<0>{}); ldt(IC<1>{});
  STAMP(15);
  constexpr float QS = 0.25f * 1.4426950408889634f;
  const u32x4 QA = pk8<0>(accQ, QS), QB = pk8<8>(accQ, QS);
  const u32x4 ones = {0x3F803F80u, 0x3F803F80u, 0x3F803F80u, 0x3F803F80u};
  u32x4 VA1 = ones, VB1 = ones;
  swap16(VA, VA1); swap16(VB, VB1);
  const f32x16 zero = {};
  f32x16 oB = zero;
  tile_full(KB, QB, VB, VB1, mask_causal<false>(lane), oB);
  SBAR();
  ldt(IC<2>{});
  const f32x4 g0 = *(const LAS f32x4*)(sm + L_GP + xrow * 32), g1 = *(const LAS f32x4*)(sm + L_GP + xrow * 32 + 16);
  const float z = ((g0[0] + g0[1]) + (g0[2] + g0[3])) + ((g1[0] + g1[1]) + (g1[2] + g1[3]));
  const float gate = __builtin_amdgcn_rcpf(1.f + __builtin_amdgcn_exp2f(-1.4426950408889634f * z));
  const u32 o_own = WS_O + (u32)(tile * 64 + head) * 1024;
  {
    f32x16 oA = zero;
    tile_full(KA, QA, VA, VA1, mask_band<false>(lane), oA);
    SBAR();
    ldt(IC<3>{});
    STAMP(16);
    wait1<12>(AH);
    STAMP(17);
    const u32 one = tile >= 1 ? 0x3F803F80u : 0u;
    u32x4 AHV;
#pragma unroll
    for (int i = 0; i < 4; ++i) AHV[i] = (lane & 16) ? one : AH[i];
    tile_upper(AH, QA, AHV, mask_band<true>(lane), oA);
    if (__builtin_expect(__any(!row_ok(oA)), 0)) {
      const int ln = opaque(lane);
      float m = tile_max<0>(KA, QA, mask_band<false>(ln), -__builtin_inff());
      if (tile >= 1) m = tile_max<8>(AH, QA, mask_band<true>(ln), m);
      m = -xhalf_max(m);
      oA = zero;
      tile_full(KA, QA, VA, VA1, addc(mask_band<false>(ln), m), oA);
      if (tile >= 1) tile_upper(AH, QA, AHV, addc(mask_band<true>(ln), m), oA);
    }
    st16_sc1(pk8<0>(oA, gate * __builtin_amdgcn_rcpf(oA[8])), lane * 16, rW, o_own);
  }
  ldt(IC<4>{});
  sfor<0, 8>([&](auto Dd) {
    constexpr int d = Dd.v;
    constexpr int N[8] = {13, 14, 15, 16, 15, 15, 14, 14};
    wait3<N[d]>(kf[d], v0[d], v1[d]);
    if constexpr (d == 0) STAMP(18); if constexpr (d == 2) STAMP(19); if constexpr (d == 4) STAMP(20); if constexpr (d == 7) STAMP(21);
    if constexpr (d == 4) signal(0);
    if constexpr (d == 7) tile_full(kf[d], QB, v0[d], v1[d], mask_causal<true>(opaque(lane)), oB);
    else tile_full(kf[d], QB, v0[d], v1[d], zero, oB);
    SBAR();
    if constexpr (d < 3) { ldt(IC<5 + d>{}); ldwo(IC<d>{}); }
    else if constexpr (d < 6) { ldwo(IC<3 * d - 6>{}); ldwo(IC<3 * d - 5>{}); ldwo(IC<3 * d - 4>{}); }
    else if constexpr (d == 6) { ldwo(IC<12>{}); ldwo(IC<13>{}); ldwo(IC<14>{}); ldwo(IC<15>{}); }
  });
  if (__builtin_expect(__any(!row_ok(oB)), 0)) {
    const int ln = opaque(lane);
    float m = tile_max<0>(KB, QB, mask_causal<false>(ln), -__builtin_inff());
    sfor<0, 8>([&](auto Dd) { constexpr int d = Dd.v; if (tile - d - 1 >= 0) m = tile_max<0>(kf[d], QB, d == 7 ? mask_causal<true>(ln) : zero, m); });
    m = -xhalf_max(m);
    oB = zero;
    tile_full(KB, QB, VB, VB1, addc(mask_causal<false>(ln), m), oB);
    const f32x16 cm = addc(zero, m);
    sfor<0, 8>([&](auto Dd) { constexpr int d = Dd.v; if (tile - d - 1 >= 0) tile_full(kf[d], QB, v0[d], v1[d], d == 7 ? addc(mask_causal<true>(ln), m) : cm, oB); });
  }
  STAMP(22);
  st16_sc1(pk8<0>(oB, (1.f - gate) * __builtin_amdgcn_rcpf(oB[8])), lane * 16, rW, o_own + 32 * 1024);
  waitv<0>();
  signal(1);
  STAMP(23);
  const int tg = w & 3, kh = w >> 2;
  const u32 vo = (u32)(h * 1024 + (g & 1) * 512 + ((tg & 1) * 16 + i16) * 16);
  const u32 so = WS_O + (u32)((2 * tb + (tg >> 1)) * 64 + 16 * kh) * 1024;
  u32x4 of[8];
  poll(cons_off + WS_F, rW, ctag, (lane >> 2) == 2 + kh);
  sfor<0, 8>([&](auto M) { constexpr int m = M.v; of[m] = ld16_sc1(vo, rW, so + m * 2048); });
  bar();
  STAMP(24);
  const LAS char* ol[4];
#pragma unroll
  for (int t = 0; t < 4; ++t) ol[t] = sm + L_W + kh * 32768 + (16 * h + 4 * (g & 1) + (i16 >> 2)) * 128 + ((t ^ (2 * (g & 1) + (i16 >> 3))) * 32) + (i16 & 3) * 8;
  f32x4 acc[4] = {};
  u32x4 wf[8][4];
  auto rdw = [&](int base) {
#pragma unroll
    for (int m = 0; m < 8; ++m)
#pragma unroll
      for (int t = 0; t < 4; ++t) wf[m][t] = ldtr(ol[t] + base + m * 4096, 1024);
  };
  auto mmw = [&]() {
    sfor<0, 8>([&](auto M) {
      constexpr int m = M.v;
      wait1<7 - m>(of[m]);
#pragma unroll
      for (int t = 0; t < 4; ++t) acc[t] = mf16(wf[m][t], of[m], acc[t]);
    });
  };
  rdw(0);
  mmw();
  SBAR();
#pragma unroll
  for (int t = 0; t < 4; ++t) ol[t] = opaque(ol[t] + 65536);
  rdw(0);
  SBAR();
  STAMP(25);
  poll(cons_off + WS_F, rW, ctag, (lane >> 2) == 4 + kh);
  STAMP(26);
  sfor<0, 8>([&](auto M) { constexpr int m = M.v; of[m] = ld16_sc1(vo, rW, so + 32768 + m * 2048); });
  mmw();
  STAMP(28);
  *(LAS f32x4*)(sm + L_X + (w * 2 + 0) * 1024 + lane * 16) = kh ? acc[0] : acc[2];
  *(LAS f32x4*)(sm + L_X + (w * 2 + 1) * 1024 + lane * 16) = kh ? acc[1] : acc[3];
  asm volatile("s_waitcnt lgkmcnt(0)\n\ts_barrier" ::: "memory");
  {
    const f32x4 a = (kh ? acc[2] : acc[0]) + *(const LAS f32x4*)(sm + L_X + ((w ^ 4) * 2 + 0) * 1024 + lane * 16);
    const f32x4 c = (kh ? acc[3] : acc[1]) + *(const LAS f32x4*)(sm + L_X + ((w ^ 4) * 2 + 1) * 1024 + lane * 16);
    auto r0 = __builtin_amdgcn_permlane16_swap(pk2(a[0], a[1]), pk2(c[0], c[1]), false, false);
    auto r1 = __builtin_amdgcn_permlane16_swap(pk2(a[2], a[3]), pk2(c[2], c[3]), false, false);
    const u32x4 v = {r0[0], r1[0], r0[1], r1[1]};
    *(u32x4*)(out + (tb * 64 + tg * 16 + i16) * 512 + hg * 64 + 32 * kh + 16 * (g & 1) + 8 * h) = v;
  }
  if (w == 0) st8_sc1(ctag, cons_off, rW, WS_E);
}

extern "C" void kernel_launch(void* const* d_in, const int* in_sizes, int n_in, void* d_out, int out_size, void* d_ws, size_t ws_size,
                              hipStream_t stream) {
  gated_mix_kernel<<<256, 512, 0, stream>>>((const u16*)d_in[0], (const u16*)d_in[1], (const u16*)d_in[2], (const u16*)d_in[4],
                                            (const u16*)d_in[5], (const u16*)d_in[6], (u16*)d_out, (char*)d_ws);
}
```

```cpp
#include <hip/hip_runtime.h>
#include <hip/hip_bf16.h>

typedef unsigned short u16;
typedef unsigned u32;
typedef unsigned long long u64;
typedef short bf16x8 __attribute__((ext_vector_type(8)));
typedef short s16x4 __attribute__((ext_vector_type(4)));
typedef __bf16 bf16x2 __attribute__((ext_vector_type(2)));
typedef float f32x2 __attribute__((ext_vector_type(2)));
typedef float f32x4 __attribute__((ext_vector_type(4)));
typedef float f32x16 __attribute__((ext_vector_type(16)));
typedef unsigned u32x2 __attribute__((ext_vector_type(2)));
typedef unsigned u32x4 __attribute__((ext_vector_type(4)));
typedef __amdgpu_buffer_rsrc_t rsrc_t;
#define LAS __attribute__((address_space(3)))
#define DI __device__ __forceinline__
#define AI __attribute__((always_inline))
#define SBAR() __builtin_amdgcn_sched_barrier(0)

#define PB_ISS 1
#define PB_CHK 5
#define STAMPS 0

constexpr u32 WS_F = 0;
constexpr u32 WS_E = 65536;
constexpr u32 KVS = 2176;
constexpr u32 WS_KV = 131072;
constexpr u32 WS_O = WS_KV + 64 * 32 * KVS;
constexpr u32 WS_Z = WS_O + 64 * 2 * 32 * 1024;
constexpr u32 WS_END = WS_Z + 32 * 1024;
constexpr u32 WS_ST = WS_END;
constexpr u32 OOB = 0x7F000000u;
constexpr u64 TAGC = 0x9E3779B97F4A7C15ull;
constexpr u32 ONE2 = 0x3F803F80u, PZN = 0xFFFFFFFFu;
constexpr u32 L_W = 0;
constexpr u32 L_X = 98304;
constexpr u32 L_EX = L_X;
constexpr u32 L_XH = L_X + 16384;
constexpr u32 L_RED = L_X + 32768;
constexpr u32 L_GW = 147456;
constexpr u32 L_M = 148480;
constexpr u32 L_CNT = 162816;
constexpr u32 L_END = 162832;

template <int N> struct IC { static constexpr int v = N; };
template <int I, int N, class F> DI void sfor(F&& f) { if constexpr (I < N) { f(IC<I>{}); sfor<I + 1, N>(f); } }

DI rsrc_t mkrs(const void* p, u32 bytes) { return __builtin_amdgcn_make_buffer_rsrc((void*)p, (short)0, (int)bytes, 0x00020000); }
DI void dma(u32 lds, u32 voff, rsrc_t rs, u32 soff) {
  asm volatile("s_mov_b32 m0, %0\n\ts_nop 0\n\tbuffer_load_dwordx4 %1, %2, %3 offen lds" ::"s"(lds), "v"(voff), "s"(rs), "s"(soff) : "m0", "memory");
}
template <int N> DI void wait_bar() { asm volatile("s_waitcnt vmcnt(%0) lgkmcnt(0)\n\ts_barrier" ::"i"(N) : "memory"); }
DI u32x4 ld16_sc1(u32 voff, rsrc_t rs, u32 soff) { u32x4 r; asm volatile("buffer_load_dwordx4 %0, %1, %2, %3 offen sc1" : "=v"(r) : "v"(voff), "s"(rs), "s"(soff) : "memory"); return r; }
DI u32x2 ld8_sc1(u32 voff, rsrc_t rs, u32 soff) { u32x2 r; asm volatile("buffer_load_dwordx2 %0, %1, %2, %3 offen sc1" : "=v"(r) : "v"(voff), "s"(rs), "s"(soff) : "memory"); return r; }
DI void st16_sc1(u32x4 v, u32 voff, rsrc_t rs, u32 soff) { asm volatile("buffer_store_dwordx4 %0, %1, %2, %3 offen sc1\n\ts_nop 1" ::"v"(v), "v"(voff), "s"(rs), "s"(soff) : "memory"); }
DI void st16(u32x4 v, u32 voff, rsrc_t rs, u32 soff) { asm volatile("buffer_store_dwordx4 %0, %1, %2, %3 offen\n\ts_nop 1" ::"v"(v), "v"(voff), "s"(rs), "s"(soff) : "memory"); }
DI void st4_sc1(u32 v, u32 voff, rsrc_t rs, u32 soff) { asm volatile("buffer_store_dword %0, %1, %2, %3 offen sc1" ::"v"(v), "v"(voff), "s"(rs), "s"(soff) : "memory"); }
DI u32 ld4_sc1(u32 voff, rsrc_t rs, u32 soff) { u32 r; asm volatile("buffer_load_dword %0, %1, %2, %3 offen sc1" : "=v"(r) : "v"(voff), "s"(rs), "s"(soff) : "memory"); return r; }
template <int N> DI void wait0(u32& a) { asm volatile("s_waitcnt vmcnt(%1)" : "+v"(a) : "i"(N)); }
template <int M> DI void wait_a_load_b(u32x4& a, u32x4& b, u32 late, u32 voff, rsrc_t rs, u32 soff) {
  asm volatile("s_cmp_lg_u32 %2, 0\n\ts_cbranch_scc1 1f\n\ts_waitcnt vmcnt(7)\n\tbuffer_load_dwordx4 %1, %3, %4, %5 offen sc1\n\ts_branch 2f\n1:\n\ts_waitcnt vmcnt(%6)\n2:"
               : "+v"(a), "=&v"(b) : "s"(late), "v"(voff), "s"(rs), "s"(soff), "i"(7 - M) : "scc", "memory");
}
DI void st8_sc1(u32x2 v, u32 voff, rsrc_t rs, u32 soff) { asm volatile("buffer_store_dwordx2 %0, %1, %2, %3 offen sc1" ::"v"(v), "v"(voff), "s"(rs), "s"(soff) : "memory"); }
DI void st8(u32x2 v, u32 voff, rsrc_t rs, u32 soff) { asm volatile("buffer_store_dwordx2 %0, %1, %2, %3 offen" ::"v"(v), "v"(voff), "s"(rs), "s"(soff) : "memory"); }
DI void pin4(u32x4& a) { asm volatile("" : "+v"(a)); }
DI void pin(u32x2& a, u32x2& b) { asm volatile("" : "+v"(a), "+v"(b)); }
template <int N> DI void wait1(u32x4& a) { asm volatile("s_waitcnt vmcnt(%1)" : "+v"(a) : "i"(N)); }
template <int N> DI void wait3(u32x4& a, u32x4& b, u32x4& c) { asm volatile("s_waitcnt vmcnt(%3)" : "+v"(a), "+v"(b), "+v"(c) : "i"(N)); }
DI void ld_kv(u32x4& k, u32x4& v0, u32x4& v1, u32 vk, u32 va, u32 vb, rsrc_t rs, u32 so) {
  asm volatile("buffer_load_dwordx4 %0, %3, %6, %7 offen sc1\n\tbuffer_load_dwordx4 %1, %4, %6, %7 offen sc1\n\tbuffer_load_dwordx4 %2, %5, %6, %7 offen sc1"
               : "=&v"(k), "=&v"(v0), "=&v"(v1) : "v"(vk), "v"(va), "v"(vb), "s"(rs), "s"(so) : "memory");
}
DI void MF(f32x4& c, u32x4 a, u32x4 b) { asm volatile("v_mfma_f32_16x16x32_bf16 %0, %1, %2, %0" : "+v"(c) : "v"(a), "v"(b)); }
DI void MF0(f32x4& c, u32x4 a, u32x4 b) { asm volatile("v_mfma_f32_16x16x32_bf16 %0, %1, %2, 0" : "=&v"(c) : "v"(a), "v"(b)); }
DI void MF(f32x16& c, u32x4 a, u32x4 b) { asm volatile("v_mfma_f32_32x32x16_bf16 %0, %1, %2, %0" : "+v"(c) : "v"(a), "v"(b)); }
DI void MF0(f32x16& c, u32x4 a, u32x4 b) { asm volatile("v_mfma_f32_32x32x16_bf16 %0, %1, %2, 0" : "=&v"(c) : "v"(a), "v"(b)); }
DI void s16(u32& a, u32& b) { auto r = __builtin_amdgcn_permlane16_swap(a, b, false, false); a = r[0]; b = r[1]; asm volatile("" : "+v"(a), "+v"(b)); }
DI void s32(u32& a, u32& b) { auto r = __builtin_amdgcn_permlane32_swap(a, b, false, false); a = r[0]; b = r[1]; asm volatile("" : "+v"(a), "+v"(b)); }
DI void sel1(u32& r, u64 m, u32 t, u32 f) { asm volatile("v_cndmask_b32_e64 %0, %1, %2, %3" : "=v"(r) : "v"(f), "v"(t), "s"(m)); }
template <int N> DI void waitv() { asm volatile("s_waitcnt vmcnt(%0)" ::"i"(N) : "memory"); }
DI void nop3() { asm volatile("s_nop 2" ::: "memory"); }
DI void bar() { asm volatile("s_barrier" ::: "memory"); }
DI u32x2 tagof(u32x2 e) { const u64 t = (((u64)e[1] << 32) | e[0]) + TAGC; return u32x2{(u32)t, (u32)(t >> 32)}; }
DI const LAS char* opaque(const LAS char* p) { asm volatile("" : "+v"(p)); return p; }
DI u64 rtime() { u64 t; asm volatile("s_memrealtime %0\n\ts_waitcnt lgkmcnt(0)" : "=s"(t)::"memory"); return t; }
DI u32 pk2(float a, float b) { f32x2 v = {a, b}; return __builtin_bit_cast(u32, __builtin_convertvector(v, bf16x2)); }
template <int B> DI u32x4 pk8(const f32x16& a, float s) { return u32x4{pk2(a[B] * s, a[B + 1] * s), pk2(a[B + 2] * s, a[B + 3] * s), pk2(a[B + 4] * s, a[B + 5] * s), pk2(a[B + 6] * s, a[B + 7] * s)}; }
template <int B> DI u32x4 pk8(const f32x16& a) { return u32x4{pk2(a[B], a[B + 1]), pk2(a[B + 2], a[B + 3]), pk2(a[B + 4], a[B + 5]), pk2(a[B + 6], a[B + 7])}; }
DI float dot2(u32 a, u32 b, float c) { return __builtin_amdgcn_fdot2_f32_bf16(__builtin_bit_cast(bf16x2, a), __builtin_bit_cast(bf16x2, b), c, false); }
DI void swap16(u32x4& a, u32x4& b) {
#pragma unroll
  for (int i = 0; i < 4; ++i) { auto r = __builtin_amdgcn_permlane16_swap(a[i], b[i], false, false); a[i] = r[0]; b[i] = r[1]; }
}
DI f32x16 mf32(u32x4 a, u32x4 b, f32x16 c) { return __builtin_amdgcn_mfma_f32_32x32x16_bf16(__builtin_bit_cast(bf16x8, a), __builtin_bit_cast(bf16x8, b), c, 0, 0, 0); }
DI f32x4 mf16(u32x4 a, u32x4 b, f32x4 c) { return __builtin_amdgcn_mfma_f32_16x16x32_bf16(__builtin_bit_cast(bf16x8, a), __builtin_bit_cast(bf16x8, b), c, 0, 0, 0); }
DI u32x4 lds16(const LAS char* p) { return *(const LAS u32x4*)p; }
DI u32x2 tr8(const LAS char* p) { return __builtin_bit_cast(u32x2, __builtin_amdgcn_ds_read_tr16_b64_v4i16((LAS s16x4*)p)); }
DI u32x4 cat(u32x2 a, u32x2 b) { return u32x4{a[0], a[1], b[0], b[1]}; }
template <u32 OFF> DI void dma2(u32 lds, u32 v0, u32 v1, rsrc_t r0, rsrc_t r1, u32 soff) {
  asm volatile("s_add_u32 m0, %0, %1\n\ts_nop 0\n\tbuffer_load_dwordx4 %2, %4, %6 offen lds\n\tbuffer_load_dwordx4 %3, %5, %6 offen offset:1024 lds"
               ::"s"(lds), "i"(OFF), "v"(v0), "v"(v1), "s"(r0), "s"(r1), "s"(soff) : "m0", "scc", "memory");
}
template <u32 OFF> DI void dma1(u32 lds, u32 voff, rsrc_t r0, u32 soff) {
  asm volatile("s_add_u32 m0, %0, %1\n\ts_nop 0\n\tbuffer_load_dwordx4 %2, %3, %4 offen lds" ::"s"(lds), "i"(OFF), "v"(voff), "s"(r0), "s"(soff) : "m0", "scc", "memory");
}
DI u32 touch(u32 voff, rsrc_t rs, u32 soff) { u32 r; asm volatile("buffer_load_dword %0, %1, %2, %3 offen" : "=v"(r) : "v"(voff), "s"(rs), "s"(soff) : "memory"); return r; }
DI void asm_pin2f(float& a, float& b) { asm volatile("" : "+v"(a), "+v"(b)); }
DI u32x4 sel4(u64 m, u32x4 t, u32x4 f) {
  u32x4 r;
  asm("v_cndmask_b32_e64 %0, %4, %8, %12\n\tv_cndmask_b32_e64 %1, %5, %9, %12\n\tv_cndmask_b32_e64 %2, %6, %10, %12\n\tv_cndmask_b32_e64 %3, %7, %11, %12"
      : "=&v"(r[0]), "=&v"(r[1]), "=&v"(r[2]), "=&v"(r[3])
      : "v"(f[0]), "v"(f[1]), "v"(f[2]), "v"(f[3]), "v"(t[0]), "v"(t[1]), "v"(t[2]), "v"(t[3]), "s"(m));
  return r;
}
DI void pin1(u32& a) { asm volatile("" : "+v"(a)); }
DI void pin2(u32x2& a) { asm volatile("" : "+v"(a)); }
DI void pin16(f32x16& a) { asm volatile("" : "+v"(a)); }
DI void pinf1(float& a) { asm volatile("" : "+v"(a)); }
DI void pinf(f32x4& a) { asm volatile("" : "+v"(a)); }
DI void sw16(u32x2& a, u32x2& b) {
#pragma unroll
  for (int i = 0; i < 2; ++i) { auto r = __builtin_amdgcn_permlane16_swap(a[i], b[i], false, false); a[i] = r[0]; b[i] = r[1]; }
}
DI void sw32(u32x2& a, u32x2& b) {
#pragma unroll
  for (int i = 0; i < 2; ++i) { auto r = __builtin_amdgcn_permlane32_swap(a[i], b[i], false, false); a[i] = r[0]; b[i] = r[1]; }
}
DI void cv(u32& r, float a, float b) { r = pk2(a, b); asm volatile("" : "+v"(r)); }
DI u32x2 pk4(f32x4 a) { return u32x2{pk2(a[0], a[1]), pk2(a[2], a[3])}; }
DI u32x2 pk4(f32x4 a, float s) { return u32x2{pk2(a[0] * s, a[1] * s), pk2(a[2] * s, a[3] * s)}; }
DI u32x4 frag_kq(u32x2 a, u32x2 b) {
  auto r0 = __builtin_amdgcn_permlane16_swap(a[0], b[0], false, false), r1 = __builtin_amdgcn_permlane16_swap(a[1], b[1], false, false);
  return u32x4{r0[0], r1[0], r0[1], r1[1]};
}
DI u32x4 frag_v(u32x2 a, u32x2 b) {
  auto r0 = __builtin_amdgcn_permlane16_swap(a[0], b[0], false, false), r1 = __builtin_amdgcn_permlane16_swap(a[1], b[1], false, false);
  auto s0 = __builtin_amdgcn_permlane32_swap(r0[0], r0[1], false, false), s1 = __builtin_amdgcn_permlane32_swap(r1[0], r1[1], false, false);
  return u32x4{s0[0], s1[0], s0[1], s1[1]};
}
DI u32x4 nopz(u32x4 v) { return u32x4{min(v[0], PZN - 1), min(v[1], PZN - 1), min(v[2], PZN - 1), min(v[3], PZN - 1)}; }
DI u32 umax4(u32 m, u32x4 v) { return max(max(m, v[0]), max(max(v[1], v[2]), v[3])); }
DI f32x16 cat16(f32x4 a, f32x4 b, f32x4 c, f32x4 d) { return f32x16{a[0], a[1], a[2], a[3], b[0], b[1], b[2], b[3], c[0], c[1], c[2], c[3], d[0], d[1], d[2], d[3]}; }
template <int N> DI void wait2(u32x2& a) { asm volatile("s_waitcnt vmcnt(%1)" : "+v"(a) : "i"(N)); }
DI u64 flags_off(u32x2 f, u32x2 tag) { return __builtin_amdgcn_ballot_w64((f[0] != tag[0]) | (f[1] != tag[1])); }
DI u32x4 ldtr(const LAS char* p, u32 second) {
  u32x2 lo = __builtin_bit_cast(u32x2, __builtin_amdgcn_ds_read_tr16_b64_v4i16((LAS s16x4*)p));
  u32x2 hi = __builtin_bit_cast(u32x2, __builtin_amdgcn_ds_read_tr16_b64_v4i16((LAS s16x4*)(p + second)));
  return u32x4{lo[0], lo[1], hi[0], hi[1]};
}
DI float xhalf_max(float v) { auto r = __builtin_amdgcn_permlane32_swap(__float_as_uint(v), __float_as_uint(v), false, false); return fmaxf(__uint_as_float(r[0]), __uint_as_float(r[1])); }
DI void poll(u32 voff, rsrc_t rs, u32x2 tag, u64 need) {
#pragma unroll 1
  for (int spin = 0; spin < (1 << 16); ++spin) {
    u32x2 f;
    asm volatile("buffer_load_dwordx2 %0, %1, %2, 0 offen sc1\n\ts_waitcnt vmcnt(0)" : "=&v"(f) : "v"(voff), "s"(rs) : "memory");
    if (!(flags_off(f, tag) & need)) break;
    __builtin_amdgcn_s_sleep(1);
  }
}
#define LS_LO 0
DI void pins(u64& t) { asm volatile("" : "+s"(t)); }
DI u64 ltime() { u64 t; asm volatile("s_memrealtime %0" : "=s"(t)::"memory"); return t; }
#define STAMP(i)

DI void tile_full(u32x4 k, u32x4 q, u32x4 v0, u32x4 v1, const f32x16& c, f32x16& o) {
  f32x16 s = mf32(k, q, c);
#pragma unroll
  for (int i = 0; i < 16; ++i) s[i] = __builtin_amdgcn_exp2f(s[i]);
  o = mf32(v0, pk8<0>(s), o);
  o = mf32(v1, pk8<8>(s), o);
}
DI void tile_upper(u32x4 k, u32x4 q, u32x4 v1, const f32x16& c, f32x16& o) {
  f32x16 s = mf32(k, q, c);
#pragma unroll
  for (int i = 8; i < 16; ++i) s[i] = __builtin_amdgcn_exp2f(s[i]);
  o = mf32(v1, pk8<8>(s), o);
}
template <int B> DI float tile_max(u32x4 k, u32x4 q, const f32x16& c, float m) {
  f32x16 s = mf32(k, q, c);
#pragma unroll
  for (int i = B; i < 16; ++i) m = fmaxf(m, s[i]);
  return m;
}
DI f32x16 addc(const f32x16& c, float x) { f32x16 r;
#pragma unroll
  for (int i = 0; i < 16; ++i) r[i] = c[i] + x;
  return r; }
DI bool rows_bad(const f32x16& o) {
  const float s = ((o[0] + o[1]) + (o[2] + o[3])) + ((o[4] + o[5]) + (o[6] + o[7]));
  const bool bad = !(o[8] >= 0x1p-80f) | !(o[8] <= 0x1p100f) | !(fabsf(s) <= 0x1.fffffep127f);
  return __builtin_amdgcn_ballot_w64(bad) != 0;
}
DI void ex1(f32x16& s, int i) { float t = __builtin_amdgcn_exp2f(s[i]); asm volatile("" : "+v"(t)); s[i] = t; }
DI void ldm(f32x16& s, int q, const LAS char* p) { const f32x4 t = *(const LAS f32x4*)p; s[4 * q] = t[0]; s[4 * q + 1] = t[1]; s[4 * q + 2] = t[2]; s[4 * q + 3] = t[3]; }

__global__ __launch_bounds__(512, 2) void gated_mix_kernel(const u16* __restrict__ x, const u16* __restrict__ wa, const u16* __restrict__ woa,
                                                           const u16* __restrict__ wb, const u16* __restrict__ wob, const u16* __restrict__ gw,
                                                           u16* __restrict__ out, char* __restrict__ ws) {
  __shared__ __attribute__((aligned(1024))) char smem_[L_END];
  const LAS char* sm = (const LAS char*)smem_;
  const u32 smb = (u32)(size_t)sm;
  const int lane = threadIdx.x & 63;
  const int w = __builtin_amdgcn_readfirstlane(threadIdx.x >> 6);
  STAMP(0);
  asm volatile("" ::"s"(x), "s"(wa), "s"(woa), "s"(wb), "s"(wob), "s"(gw), "s"(out), "s"(ws));
  const int b = blockIdx.x, xcd = b & 7, rr = b >> 3;
  const int tb = (xcd & 1) * 16 + (rr & 15), hg = (xcd >> 1) * 2 + (rr >> 4);
  const int wg = tb * 8 + hg;
  const int ha = (hg + 4) & 7;
  const int to = xcd * 4 + (rr & 3), cg = rr >> 2;
  const int rot = 2 * (rr & 3) + 4 * (rr >> 4) + 2 * ((xcd >> 1) & 1) + (xcd >> 2);
  const int tq = w & 1, hh = w >> 1, head = hg * 4 + hh, tile = tb * 2 + tq;
  const int h = lane >> 5, g = lane >> 4, i16 = lane & 15;

  const rsrc_t rX = mkrs(x, 2048 * 512 * 2), rA = mkrs(wa, 512 * 1536 * 2), rB = mkrs(wb, 512 * 1536 * 2), rW = mkrs(ws, WS_END);
  const rsrc_t rOA = mkrs(woa, 512 * 512 * 2), rOB = mkrs(wob, 512 * 512 * 2);

  const u32 vX = (u32)((tb * 64 + 8 * w + (lane >> 3)) * 1024 + (((lane & 7) ^ ((4 * (w & 1) + g) & 7)) * 16));
  const u32 vW0 = (u32)((lane >> 3) * 3072 + (((lane & 7) ^ ((2 * (w & 1) + (g & 1)) << 1)) * 16));
  const u32 vWA = vW0 + ha * 128, vWB = vW0 + hg * 128, vKA = vWA + 1024, vKB = vWB + 1024;
  const rsrc_t rBq = mkrs(wb - 512, 512 * 1536 * 2 + 1024);
  auto kst = [&](int i) AI { return (u32)((i + rot) & 7); };
  u32 sk[8];
#pragma unroll
  for (int c = 0; c < 8; ++c) sk[c] = (u32)(8 * w * 3072) + kst(c) * (64 * 3072);
  const u32 lw = smb + w * 2048, lx = smb + w * 1024;
  auto issue_w = [&](auto I, auto U) AI {
    constexpr int s = I.v, u = U.v, ps = s >> 2;
    if constexpr (s < 12) dma2<(s == 8 ? L_X + u * 32768 : L_W + (s % 3) * 32768 + u * 16384)>(lw, ps == 0 ? vKB : ps == 1 ? vKA : vWA, ps == 0 ? vKB : ps == 1 ? vKA : vWB, ps == 0 ? rB : rA, ps == 0 ? rB : ps == 1 ? rA : rBq, sk[2 * (s & 3) + u]);
  };
  auto issue_x = [&](auto I, auto U) AI {
    constexpr int s = I.v, u = U.v;
    if constexpr (s < 4) dma1<L_X + (s % 3) * 16384 + u * 8192>(lx, vX, rX, kst(2 * s + u) * 128);
    if constexpr (s == 4) {
      const u32 vH = (u32)((tb * 64 - 16 + 8 * u + (lane >> 3)) * 1024 + (((lane & 7) ^ (4 * u + g)) * 16));
      dma1<L_XH + u * 1024>(lw, vH, rX, tb > 0 ? kst(w) * 128 : OOB);
    }
  };
  constexpr IC<0> c0; constexpr IC<1> c1; constexpr IC<2> c2; constexpr IC<3> c3;
  auto issue_all = [&](auto I) AI { issue_x(I, c0); issue_w(I, c0); issue_x(I, c1); issue_w(I, c1); };
  if (w == 7) dma(smb + L_GW, lane * 16, mkrs(gw, 1024), 0);
  issue_all(c0);
  SBAR();
  const u32 cons_off = lane < 24 ? (u32)(lane < 8 ? wg * 256 + lane * 8 : (to * 8 + cg) * 256 + 64 + lane * 8) : OOB;
  u32 prod_off = OOB;
  if (lane < 4 && tb + lane + 1 < 32) prod_off = (u32)(((tb + lane + 1) * 8 + hg) * 256 + lane * 8);
  if (lane >= 8 && lane < 24) prod_off = (u32)((tb * 8 + (lane & 7)) * 256 + 64 + ((lane & 24) + hg) * 8);
  if (threadIdx.x == 0) *(LAS u32*)(sm + L_CNT) = 0;
  {
    const int qi = lane & 31;
    const u32 causal = (2u << qi) - 1u;
    const u64 band = (u64)0x5555u << (qi + 18);
    const u32 keep = w < 2 ? causal : w < 4 ? ~causal : w < 6 ? (u32)(band >> 32) : (u32)band;
    const u32 n = ~keep >> (4 * h + ((w & 1) || w == 6 ? 16 : 0));
    u32x4 m0, m1;
#pragma unroll
    for (int r = 0; r < 4; ++r) { m0[r] = (u32)((int)(n << (31 - r)) >> 31) & 0xFF800000u; m1[r] = (u32)((int)(n << (23 - r)) >> 31) & 0xFF800000u; }
    *(LAS u32x4*)(sm + L_M + min(w, 6) * 2048 + lane * 16) = m0;
    *(LAS u32x4*)(sm + L_M + min(w, 6) * 2048 + 1024 + lane * 16) = m1;
  }

  const int rp = hh ^ (2 * tq), xr = 32 * tq + i16;
  const LAS char *xa[4], *wl[3][4], *xh[2];
#pragma unroll
  for (int i = 0; i < 4; ++i) {
    xa[i] = sm + L_X + ((i ^ rp) >> 1) * 8192 + xr * 128 + (((4 * ((i ^ rp) & 1) + g) ^ ((xr >> 1) & 7)) * 16);
    wl[0][i] = sm + L_W + (i ^ rp) * 8192 + g * 2048 + (i16 >> 2) * 128 + ((hh ^ (2 * (g & 1) + (i16 >> 3))) * 32) + (i16 & 3) * 8;
    wl[1][i] = opaque(wl[0][i] + 65536);
  }
#pragma unroll
  for (int i = 0; i < 2; ++i) xh[i] = opaque(sm + L_XH + (rp >> 1) * 2048 + i16 * 128 + (((4 * ((i ^ rp) & 1) + g) ^ (i16 >> 1)) * 16));
  const LAS char* gl = sm + L_GW + (rp & 1) * 64 + g * 16;
  const u32 oa_own = WS_O + (u32)(tile * 64 + ha * 4 + hh) * 1024, ob_own = WS_O + (u32)(tile * 64 + 32 + head) * 1024;
  const u32 kv_own = WS_KV + (u32)(tile * 32 + head) * KVS;
  u32 vV = (u32)(1024 + (g & 1) * 512 + h * 256 + i16 * 16);
  u32 vV0 = (g & 1) ? 2048u : vV, vV1 = (g & 1) ? 2048u : vV + 512, v1l = lane == 0 ? 2048u : OOB;
  u32 vL = (u32)lane * 16;
  const LAS char* ex = opaque(sm + L_EX + w * 2048 + lane * 16);
  pin1(vV); pin1(vL); pin1(vV0); pin1(vV1); pin1(v1l);
  const u32x4 ones = {ONE2, ONE2, ONE2, ONE2};
  const u32 mtq32 = __builtin_amdgcn_readfirstlane(0u - (u32)tq);
  const u64 mtq = ((u64)mtq32 << 32) | mtq32;
  const u32 vO = (u32)((lane >> 3) * 1024 + (((lane & 7) ^ ((g & 3) << 1)) * 16));
  auto ldwo = [&](auto P) AI {
    constexpr int p = P.v, br = p >> 3;
    const int q = p & 7, sl = q >> 2, i = q & 3;
    dma1<L_W + br * 65536>(smb + sl * 32768 + w * 4096 + i * 1024, vO, br ? rOB : rOA, (u32)((sl * 256 + w * 32 + i * 8) * 1024 + cg * 128));
  };
  SBAR();
  issue_all(c1);
  u32x2 ctag = ld8_sc1(cons_off, rW, WS_E);
  u32x2 ptag = ld8_sc1(prod_off, rW, WS_E);

  u32x4 xf[16][2];
  u32x2 w0[4][2], w1[4][2];
  f32x4 acc[3][2][2];
  f32x4 ach[2];
  f32x4 acg[2];
  u32x4 xhf[2], gv, e0, e1;
  u32 KA[4], KB[4], VA[4], VB[4], VA1[4], VB1[4], AK[4], AV[4], ta[4], tc[4];
  f32x4 f0, f1;
  u32x4 kf[8], v0[8], v1[8];
  u32x2 fl;
  u32 one_h, zb;
  auto ldt = [&](auto Dd) AI { constexpr int d = Dd.v; ld_kv(kf[d], v0[d], v1[d], vL, vV0, vV1, rW, tile - d - 1 >= 0 ? kv_own - (d + 1) * 32 * KVS : OOB); };
  auto v4 = [](const u32* a) AI { return u32x4{a[0], a[1], a[2], a[3]}; };
  wait_bar<8>(); STAMP(1);
  SBAR();
  w0[0][0] = tr8(wl[0][0] + 0); SBAR();
  w1[0][0] = tr8(wl[0][0] + 512); SBAR();
  xf[0][0] = lds16(xa[0] + 0); SBAR();
  xf[0][1] = lds16(xa[0] + 2048); SBAR();
  w0[0][1] = tr8(wl[0][0] + 1024); SBAR();
  w1[0][1] = tr8(wl[0][0] + 1536); SBAR();
  w0[1][0] = tr8(wl[0][1] + 0); SBAR();
  w1[1][0] = tr8(wl[0][1] + 512); SBAR();
  xf[1][0] = lds16(xa[1] + 0); SBAR();
  xf[1][1] = lds16(xa[1] + 2048); SBAR();
  w0[1][1] = tr8(wl[0][1] + 1024); SBAR();
  w1[1][1] = tr8(wl[0][1] + 1536); SBAR();
  MF0(acc[0][0][0], cat(w0[0][0], w1[0][0]), xf[0][0]); SBAR();
  gv = lds16(gl + kst(0 + (rp >> 1)) * 128); SBAR();
  w0[2][0] = tr8(wl[0][2] + 0); SBAR();
  w1[2][0] = tr8(wl[0][2] + 512); SBAR();
  issue_w(IC<2>{}, c0); SBAR();
  MF0(acc[0][0][1], cat(w0[0][0], w1[0][0]), xf[0][1]); SBAR();
  xf[2][0] = lds16(xa[2] + 0); SBAR();
  xf[2][1] = lds16(xa[2] + 2048); SBAR();
  w0[2][1] = tr8(wl[0][2] + 1024); SBAR();
  MF0(acc[0][1][0], xf[0][0], cat(w0[0][1], w1[0][1])); SBAR();
  w1[2][1] = tr8(wl[0][2] + 1536); SBAR();
  w0[3][0] = tr8(wl[0][3] + 0); SBAR();
  issue_x(IC<2>{}, c0); SBAR();
  MF0(acc[0][1][1], xf[0][1], cat(w0[0][1], w1[0][1])); SBAR();
  w1[3][0] = tr8(wl[0][3] + 512); SBAR();
  xf[3][0] = lds16(xa[3] + 0); SBAR();
  xf[3][1] = lds16(xa[3] + 2048); SBAR();
  MF(acc[0][0][0], cat(w0[1][0], w1[1][0]), xf[1][0]); SBAR();
  w0[3][1] = tr8(wl[0][3] + 1024); SBAR();
  w1[3][1] = tr8(wl[0][3] + 1536); SBAR();
  issue_w(IC<2>{}, c1); SBAR();
  MF(acc[0][0][1], cat(w0[1][0], w1[1][0]), xf[1][1]); SBAR();
  MF(acc[0][1][0], xf[1][0], cat(w0[1][1], w1[1][1])); SBAR();
  issue_x(IC<2>{}, c1); SBAR();
  MF(acc[0][1][1], xf[1][1], cat(w0[1][1], w1[1][1])); SBAR();
  MF0(acg[0], gv, xf[0][0]); SBAR();
  MF0(acg[1], gv, xf[0][1]); SBAR();
  wait_bar<8>(); STAMP(2);
  SBAR();
  MF(acc[0][0][0], cat(w0[2][0], w1[2][0]), xf[2][0]); SBAR();
  w0[0][0] = tr8(wl[0][0] + 32768); SBAR();
  w1[0][0] = tr8(wl[0][0] + 33280); SBAR();
  xf[4][0] = lds16(xa[0] + 16384); SBAR();
  MF(acc[0][0][1], cat(w0[2][0], w1[2][0]), xf[2][1]); SBAR();
  xf[4][1] = lds16(xa[0] + 18432); SBAR();
  w0[0][1] = tr8(wl[0][0] + 33792); SBAR();
  issue_w(IC<3>{}, c0); SBAR();
  MF(acc[0][1][0], xf[2][0], cat(w0[2][1], w1[2][1])); SBAR();
  w1[0][1] = tr8(wl[0][0] + 34304); SBAR();
  w0[1][0] = tr8(wl[0][1] + 32768); SBAR();
  MF(acc[0][1][1], xf[2][1], cat(w0[2][1], w1[2][1])); SBAR();
  w1[1][0] = tr8(wl[0][1] + 33280); SBAR();
  xf[5][0] = lds16(xa[1] + 16384); SBAR();
  MF(acc[0][0][0], cat(w0[3][0], w1[3][0]), xf[3][0]); SBAR();
  xf[5][1] = lds16(xa[1] + 18432); SBAR();
  w0[1][1] = tr8(wl[0][1] + 33792); SBAR();
  MF(acc[0][0][1], cat(w0[3][0], w1[3][0]), xf[3][1]); SBAR();
  w1[1][1] = tr8(wl[0][1] + 34304); SBAR();
  gv = lds16(gl + kst(2 + (rp >> 1)) * 128); SBAR();
  issue_x(IC<3>{}, c0); SBAR();
  MF(acc[0][1][0], xf[3][0], cat(w0[3][1], w1[3][1])); SBAR();
  w0[2][0] = tr8(wl[0][2] + 32768); SBAR();
  w1[2][0] = tr8(wl[0][2] + 33280); SBAR();
  MF(acc[0][1][1], xf[3][1], cat(w0[3][1], w1[3][1])); SBAR();
  xf[6][0] = lds16(xa[2] + 16384); SBAR();
  xf[6][1] = lds16(xa[2] + 18432); SBAR();
  MF(acc[0][0][0], cat(w0[0][0], w1[0][0]), xf[4][0]); SBAR();
  w0[2][1] = tr8(wl[0][2] + 33792); SBAR();
  w1[2][1] = tr8(wl[0][2] + 34304); SBAR();
  MF(acc[0][0][1], cat(w0[0][0], w1[0][0]), xf[4][1]); SBAR();
  w0[3][0] = tr8(wl[0][3] + 32768); SBAR();
  w1[3][0] = tr8(wl[0][3] + 33280); SBAR();
  issue_w(IC<3>{}, c1); SBAR();
  MF(acc[0][1][0], xf[4][0], cat(w0[0][1], w1[0][1])); SBAR();
  xf[7][0] = lds16(xa[3] + 16384); SBAR();
  xf[7][1] = lds16(xa[3] + 18432); SBAR();
  MF(acc[0][1][1], xf[4][1], cat(w0[0][1], w1[0][1])); SBAR();
  w0[3][1] = tr8(wl[0][3] + 33792); SBAR();
  w1[3][1] = tr8(wl[0][3] + 34304); SBAR();
  MF(acc[0][0][0], cat(w0[1][0], w1[1][0]), xf[5][0]); SBAR();
  MF(acc[0][0][1], cat(w0[1][0], w1[1][0]), xf[5][1]); SBAR();
  issue_x(IC<3>{}, c1); SBAR();
  MF(acc[0][1][0], xf[5][0], cat(w0[1][1], w1[1][1])); SBAR();
  MF(acc[0][1][1], xf[5][1], cat(w0[1][1], w1[1][1])); SBAR();
  MF(acg[0], gv, xf[4][0]); SBAR();
  MF(acg[1], gv, xf[4][1]); SBAR();
  wait_bar<6>(); STAMP(3);
  pin(ctag, ptag); ctag = tagof(ctag); ptag = tagof(ptag); pin(ctag, ptag);
  SBAR();
  MF(acc[0][0][0], cat(w0[2][0], w1[2][0]), xf[6][0]); SBAR();
  w0[0][0] = tr8(wl[1][0] + 0); SBAR();
  w1[0][0] = tr8(wl[1][0] + 512); SBAR();
  xf[8][0] = lds16(xa[0] + 32768); SBAR();
  MF(acc[0][0][1], cat(w0[2][0], w1[2][0]), xf[6][1]); SBAR();
  xf[8][1] = lds16(xa[0] + 34816); SBAR();
  w0[0][1] = tr8(wl[1][0] + 1024); SBAR();
  issue_w(IC<4>{}, c0); SBAR();
  MF(acc[0][1][0], xf[6][0], cat(w0[2][1], w1[2][1])); SBAR();
  w1[0][1] = tr8(wl[1][0] + 1536); SBAR();
  w0[1][0] = tr8(wl[1][1] + 0); SBAR();
  MF(acc[0][1][1], xf[6][1], cat(w0[2][1], w1[2][1])); SBAR();
  w1[1][0] = tr8(wl[1][1] + 512); SBAR();
  xf[9][0] = lds16(xa[1] + 32768); SBAR();
  MF(acc[0][0][0], cat(w0[3][0], w1[3][0]), xf[7][0]); SBAR();
  xf[9][1] = lds16(xa[1] + 34816); SBAR();
  w0[1][1] = tr8(wl[1][1] + 1024); SBAR();
  MF(acc[0][0][1], cat(w0[3][0], w1[3][0]), xf[7][1]); SBAR();
  w1[1][1] = tr8(wl[1][1] + 1536); SBAR();
  gv = lds16(gl + kst(4 + (rp >> 1)) * 128); SBAR();
  issue_x(IC<4>{}, c0); SBAR();
  MF(acc[0][1][0], xf[7][0], cat(w0[3][1], w1[3][1])); SBAR();
  w0[2][0] = tr8(wl[1][2] + 0); SBAR();
  w1[2][0] = tr8(wl[1][2] + 512); SBAR();
  MF(acc[0][1][1], xf[7][1], cat(w0[3][1], w1[3][1])); SBAR();
  xf[10][0] = lds16(xa[2] + 32768); SBAR();
  xf[10][1] = lds16(xa[2] + 34816); SBAR();
  MF(acc[0][0][0], cat(w0[0][0], w1[0][0]), xf[8][0]); SBAR();
  w0[2][1] = tr8(wl[1][2] + 1024); SBAR();
  w1[2][1] = tr8(wl[1][2] + 1536); SBAR();
  MF(acc[0][0][1], cat(w0[0][0], w1[0][0]), xf[8][1]); SBAR();
  w0[3][0] = tr8(wl[1][3] + 0); SBAR();
  w1[3][0] = tr8(wl[1][3] + 512); SBAR();
  issue_w(IC<4>{}, c1); SBAR();
  MF(acc[0][1][0], xf[8][0], cat(w0[0][1], w1[0][1])); SBAR();
  xf[11][0] = lds16(xa[3] + 32768); SBAR();
  xf[11][1] = lds16(xa[3] + 34816); SBAR();
  MF(acc[0][1][1], xf[8][1], cat(w0[0][1], w1[0][1])); SBAR();
  w0[3][1] = tr8(wl[1][3] + 1024); SBAR();
  w1[3][1] = tr8(wl[1][3] + 1536); SBAR();
  MF(acc[0][0][0], cat(w0[1][0], w1[1][0]), xf[9][0]); SBAR();
  MF(acc[0][0][1], cat(w0[1][0], w1[1][0]), xf[9][1]); SBAR();
  issue_x(IC<4>{}, c1); SBAR();
  MF(acc[0][1][0], xf[9][0], cat(w0[1][1], w1[1][1])); SBAR();
  MF(acc[0][1][1], xf[9][1], cat(w0[1][1], w1[1][1])); SBAR();
  MF(acg[0], gv, xf[8][0]); SBAR();
  MF(acg[1], gv, xf[8][1]); SBAR();
  wait_bar<6>(); STAMP(4);
  SBAR();
  MF(acc[0][0][0], cat(w0[2][0], w1[2][0]), xf[10][0]); SBAR();
  w0[0][0] = tr8(wl[0][0] + 0); SBAR();
  w1[0][0] = tr8(wl[0][0] + 512); SBAR();
  xf[12][0] = lds16(xa[0] + 0); SBAR();
  MF(acc[0][0][1], cat(w0[2][0], w1[2][0]), xf[10][1]); SBAR();
  xf[12][1] = lds16(xa[0] + 2048); SBAR();
  w0[0][1] = tr8(wl[0][0] + 1024); SBAR();
  MF(acc[0][1][0], xf[10][0], cat(w0[2][1], w1[2][1])); SBAR();
  w1[0][1] = tr8(wl[0][0] + 1536); SBAR();
  w0[1][0] = tr8(wl[0][1] + 0); SBAR();
  issue_w(IC<5>{}, c0); SBAR();
  MF(acc[0][1][1], xf[10][1], cat(w0[2][1], w1[2][1])); SBAR();
  w1[1][0] = tr8(wl[0][1] + 512); SBAR();
  xf[13][0] = lds16(xa[1] + 0); SBAR();
  MF(acc[0][0][0], cat(w0[3][0], w1[3][0]), xf[11][0]); SBAR();
  xf[13][1] = lds16(xa[1] + 2048); SBAR();
  w0[1][1] = tr8(wl[0][1] + 1024); SBAR();
  MF(acc[0][0][1], cat(w0[3][0], w1[3][0]), xf[11][1]); SBAR();
  w1[1][1] = tr8(wl[0][1] + 1536); SBAR();
  gv = lds16(gl + kst(6 + (rp >> 1)) * 128); SBAR();
  MF(acc[0][1][0], xf[11][0], cat(w0[3][1], w1[3][1])); SBAR();
  w0[2][0] = tr8(wl[0][2] + 0); SBAR();
  w1[2][0] = tr8(wl[0][2] + 512); SBAR();
  MF(acc[0][1][1], xf[11][1], cat(w0[3][1], w1[3][1])); SBAR();
  xf[14][0] = lds16(xa[2] + 0); SBAR();
  xf[14][1] = lds16(xa[2] + 2048); SBAR();
  MF(acc[0][0][0], cat(w0[0][0], w1[0][0]), xf[12][0]); SBAR();
  w0[2][1] = tr8(wl[0][2] + 1024); SBAR();
  w1[2][1] = tr8(wl[0][2] + 1536); SBAR();
  MF(acc[0][0][1], cat(w0[0][0], w1[0][0]), xf[12][1]); SBAR();
  w0[3][0] = tr8(wl[0][3] + 0); SBAR();
  w1[3][0] = tr8(wl[0][3] + 512); SBAR();
  issue_w(IC<5>{}, c1); SBAR();
  MF(acc[0][1][0], xf[12][0], cat(w0[0][1], w1[0][1])); SBAR();
  xf[15][0] = lds16(xa[3] + 0); SBAR();
  xf[15][1] = lds16(xa[3] + 2048); SBAR();
  MF(acc[0][1][1], xf[12][1], cat(w0[0][1], w1[0][1])); SBAR();
  w0[3][1] = tr8(wl[0][3] + 1024); SBAR();
  w1[3][1] = tr8(wl[0][3] + 1536); SBAR();
  MF(acc[0][0][0], cat(w0[1][0], w1[1][0]), xf[13][0]); SBAR();
  MF(acc[0][0][1], cat(w0[1][0], w1[1][0]), xf[13][1]); SBAR();
  MF(acc[0][1][0], xf[13][0], cat(w0[1][1], w1[1][1])); SBAR();
  MF(acc[0][1][1], xf[13][1], cat(w0[1][1], w1[1][1])); SBAR();
  MF(acg[0], gv, xf[12][0]); SBAR();
  MF(acg[1], gv, xf[12][1]); SBAR();
  wait_bar<4>(); STAMP(5);
  SBAR();
  MF(acc[0][0][0], cat(w0[2][0], w1[2][0]), xf[14][0]); SBAR();
  w0[0][0] = tr8(wl[0][0] + 32768); SBAR();
  w1[0][0] = tr8(wl[0][0] + 33280); SBAR();
  MF(acc[0][0][1], cat(w0[2][0], w1[2][0]), xf[14][1]); SBAR();
  xhf[0] = lds16(xh[0] + 0); SBAR();
  MF(acc[0][1][0], xf[14][0], cat(w0[2][1], w1[2][1])); SBAR();
  w0[0][1] = tr8(wl[0][0] + 33792); SBAR();
  issue_w(IC<6>{}, c0); SBAR();
  MF(acc[0][1][1], xf[14][1], cat(w0[2][1], w1[2][1])); SBAR();
  w1[0][1] = tr8(wl[0][0] + 34304); SBAR();
  MF(acc[0][0][0], cat(w0[3][0], w1[3][0]), xf[15][0]); SBAR();
  w0[1][0] = tr8(wl[0][1] + 32768); SBAR();
  MF(acc[0][0][1], cat(w0[3][0], w1[3][0]), xf[15][1]); SBAR();
  w1[1][0] = tr8(wl[0][1] + 33280); SBAR();
  MF(acc[0][1][0], xf[15][0], cat(w0[3][1], w1[3][1])); SBAR();
  xhf[1] = lds16(xh[1] + 0); SBAR();
  MF(acc[0][1][1], xf[15][1], cat(w0[3][1], w1[3][1])); SBAR();
  w0[1][1] = tr8(wl[0][1] + 33792); SBAR();
  MF0(acc[1][0][0], cat(w0[0][0], w1[0][0]), xf[0][0]); SBAR();
  w1[1][1] = tr8(wl[0][1] + 34304); SBAR();
  w0[2][0] = tr8(wl[0][2] + 32768); SBAR();
  MF0(acc[1][0][1], cat(w0[0][0], w1[0][0]), xf[0][1]); SBAR();
  w1[2][0] = tr8(wl[0][2] + 33280); SBAR();
  MF0(ach[0], cat(w0[0][0], w1[0][0]), xhf[0]); SBAR();
  w0[2][1] = tr8(wl[0][2] + 33792); SBAR();
  issue_w(IC<6>{}, c1); SBAR();
  MF0(acc[1][1][0], xf[0][0], cat(w0[0][1], w1[0][1])); SBAR();
  w1[2][1] = tr8(wl[0][2] + 34304); SBAR();
  cv(KB[0], acc[0][0][0][0], acc[0][0][0][1]); SBAR();
  MF0(acc[1][1][1], xf[0][1], cat(w0[0][1], w1[0][1])); SBAR();
  w0[3][0] = tr8(wl[0][3] + 32768); SBAR();
  cv(KB[2], acc[0][0][1][0], acc[0][0][1][1]); SBAR();
  MF0(ach[1], xhf[0], cat(w0[0][1], w1[0][1])); SBAR();
  w1[3][0] = tr8(wl[0][3] + 33280); SBAR();
  cv(KB[1], acc[0][0][0][2], acc[0][0][0][3]); SBAR();
  MF(acc[1][0][0], cat(w0[1][0], w1[1][0]), xf[1][0]); SBAR();
  w0[3][1] = tr8(wl[0][3] + 33792); SBAR();
  cv(KB[3], acc[0][0][1][2], acc[0][0][1][3]); SBAR();
  MF(acc[1][0][1], cat(w0[1][0], w1[1][0]), xf[1][1]); SBAR();
  w1[3][1] = tr8(wl[0][3] + 34304); SBAR();
  MF(ach[0], cat(w0[1][0], w1[1][0]), xhf[1]); SBAR();
  s16(KB[0], KB[2]); SBAR();
  MF(acc[1][1][0], xf[1][0], cat(w0[1][1], w1[1][1])); SBAR();
  s16(KB[1], KB[3]); SBAR();
  MF(acc[1][1][1], xf[1][1], cat(w0[1][1], w1[1][1])); SBAR();
  MF(ach[1], xhf[1], cat(w0[1][1], w1[1][1])); SBAR();
  st16_sc1(v4(KB), vL, rW, kv_own); SBAR();
  st16_sc1(ones, v1l, rW, kv_own); SBAR();
  wait_bar<6>(); STAMP(6);
  SBAR();
  MF(acc[1][0][0], cat(w0[2][0], w1[2][0]), xf[2][0]); SBAR();
  w0[0][0] = tr8(wl[1][0] + 0); SBAR();
  w1[0][0] = tr8(wl[1][0] + 512); SBAR();
  MF(acc[1][0][1], cat(w0[2][0], w1[2][0]), xf[2][1]); SBAR();
  xhf[0] = lds16(xh[0] + 4096); SBAR();
  cv(VB[0], acc[0][1][0][0], acc[0][1][0][1]); SBAR();
  MF(acc[1][1][0], xf[2][0], cat(w0[2][1], w1[2][1])); SBAR();
  w0[0][1] = tr8(wl[1][0] + 1024); SBAR();
  issue_w(IC<7>{}, c0); SBAR();
  MF(acc[1][1][1], xf[2][1], cat(w0[2][1], w1[2][1])); SBAR();
  w1[0][1] = tr8(wl[1][0] + 1536); SBAR();
  cv(VB[2], acc[0][1][1][0], acc[0][1][1][1]); SBAR();
  MF(acc[1][0][0], cat(w0[3][0], w1[3][0]), xf[3][0]); SBAR();
  w0[1][0] = tr8(wl[1][1] + 0); SBAR();
  cv(VB[1], acc[0][1][0][2], acc[0][1][0][3]); SBAR();
  MF(acc[1][0][1], cat(w0[3][0], w1[3][0]), xf[3][1]); SBAR();
  w1[1][0] = tr8(wl[1][1] + 512); SBAR();
  cv(VB[3], acc[0][1][1][2], acc[0][1][1][3]); SBAR();
  MF(acc[1][1][0], xf[3][0], cat(w0[3][1], w1[3][1])); SBAR();
  xhf[1] = lds16(xh[1] + 4096); SBAR();
  MF(acc[1][1][1], xf[3][1], cat(w0[3][1], w1[3][1])); SBAR();
  w0[1][1] = tr8(wl[1][1] + 1024); SBAR();
  MF(acc[1][0][0], cat(w0[0][0], w1[0][0]), xf[4][0]); SBAR();
  w1[1][1] = tr8(wl[1][1] + 1536); SBAR();
  w0[2][0] = tr8(wl[1][2] + 0); SBAR();
  MF(acc[1][0][1], cat(w0[0][0], w1[0][0]), xf[4][1]); SBAR();
  w1[2][0] = tr8(wl[1][2] + 512); SBAR();
  MF(ach[0], cat(w0[0][0], w1[0][0]), xhf[0]); SBAR();
  w0[2][1] = tr8(wl[1][2] + 1024); SBAR();
  issue_w(IC<7>{}, c1); SBAR();
  MF(acc[1][1][0], xf[4][0], cat(w0[0][1], w1[0][1])); SBAR();
  w1[2][1] = tr8(wl[1][2] + 1536); SBAR();
  MF(acc[1][1][1], xf[4][1], cat(w0[0][1], w1[0][1])); SBAR();
  w0[3][0] = tr8(wl[1][3] + 0); SBAR();
  MF(ach[1], xhf[0], cat(w0[0][1], w1[0][1])); SBAR();
  w1[3][0] = tr8(wl[1][3] + 512); SBAR();
  MF(acc[1][0][0], cat(w0[1][0], w1[1][0]), xf[5][0]); SBAR();
  w0[3][1] = tr8(wl[1][3] + 1024); SBAR();
  MF(acc[1][0][1], cat(w0[1][0], w1[1][0]), xf[5][1]); SBAR();
  w1[3][1] = tr8(wl[1][3] + 1536); SBAR();
  MF(ach[0], cat(w0[1][0], w1[1][0]), xhf[1]); SBAR();
  s16(VB[0], VB[2]); SBAR();
  MF(acc[1][1][0], xf[5][0], cat(w0[1][1], w1[1][1])); SBAR();
  s16(VB[1], VB[3]); SBAR();
  MF(acc[1][1][1], xf[5][1], cat(w0[1][1], w1[1][1])); SBAR();
  s32(VB[0], VB[2]); SBAR();
  MF(ach[1], xhf[1], cat(w0[1][1], w1[1][1])); SBAR();
  s32(VB[1], VB[3]); SBAR();
  st16_sc1(v4(VB), vV, rW, kv_own); SBAR();
  wait_bar<7>(); STAMP(7);
  SBAR();
  MF(acc[1][0][0], cat(w0[2][0], w1[2][0]), xf[6][0]); SBAR();
  w0[0][0] = tr8(wl[0][0] + 0); SBAR();
  w1[0][0] = tr8(wl[0][0] + 512); SBAR();
  MF(acc[1][0][1], cat(w0[2][0], w1[2][0]), xf[6][1]); SBAR();
  xhf[0] = lds16(xh[0] + 8192); SBAR();
  sel1(zb, 0xFFFF0000FFFF0000ull, __float_as_uint(acg[1][0]), __float_as_uint(acg[0][0])); SBAR();
  MF(acc[1][1][0], xf[6][0], cat(w0[2][1], w1[2][1])); SBAR();
  w0[0][1] = tr8(wl[0][0] + 1024); SBAR();
  issue_w(IC<8>{}, c0); SBAR();
  MF(acc[1][1][1], xf[6][1], cat(w0[2][1], w1[2][1])); SBAR();
  w1[0][1] = tr8(wl[0][0] + 1536); SBAR();
  VB1[0] = ONE2; pin1(VB1[0]); SBAR();
  MF(acc[1][0][0], cat(w0[3][0], w1[3][0]), xf[7][0]); SBAR();
  w0[1][0] = tr8(wl[0][1] + 0); SBAR();
  VB1[1] = ONE2; pin1(VB1[1]); SBAR();
  MF(acc[1][0][1], cat(w0[3][0], w1[3][0]), xf[7][1]); SBAR();
  w1[1][0] = tr8(wl[0][1] + 512); SBAR();
  VB1[2] = ONE2; pin1(VB1[2]); SBAR();
  MF(acc[1][1][0], xf[7][0], cat(w0[3][1], w1[3][1])); SBAR();
  xhf[1] = lds16(xh[1] + 8192); SBAR();
  VB1[3] = ONE2; pin1(VB1[3]); SBAR();
  MF(acc[1][1][1], xf[7][1], cat(w0[3][1], w1[3][1])); SBAR();
  w0[1][1] = tr8(wl[0][1] + 1024); SBAR();
  MF(acc[1][0][0], cat(w0[0][0], w1[0][0]), xf[8][0]); SBAR();
  w1[1][1] = tr8(wl[0][1] + 1536); SBAR();
  w0[2][0] = tr8(wl[0][2] + 0); SBAR();
  MF(acc[1][0][1], cat(w0[0][0], w1[0][0]), xf[8][1]); SBAR();
  w1[2][0] = tr8(wl[0][2] + 512); SBAR();
  MF(ach[0], cat(w0[0][0], w1[0][0]), xhf[0]); SBAR();
  w0[2][1] = tr8(wl[0][2] + 1024); SBAR();
  issue_w(IC<8>{}, c1); SBAR();
  MF(acc[1][1][0], xf[8][0], cat(w0[0][1], w1[0][1])); SBAR();
  w1[2][1] = tr8(wl[0][2] + 1536); SBAR();
  MF(acc[1][1][1], xf[8][1], cat(w0[0][1], w1[0][1])); SBAR();
  w0[3][0] = tr8(wl[0][3] + 0); SBAR();
  MF(ach[1], xhf[0], cat(w0[0][1], w1[0][1])); SBAR();
  w1[3][0] = tr8(wl[0][3] + 512); SBAR();
  MF(acc[1][0][0], cat(w0[1][0], w1[1][0]), xf[9][0]); SBAR();
  w0[3][1] = tr8(wl[0][3] + 1024); SBAR();
  MF(acc[1][0][1], cat(w0[1][0], w1[1][0]), xf[9][1]); SBAR();
  w1[3][1] = tr8(wl[0][3] + 1536); SBAR();
  MF(ach[0], cat(w0[1][0], w1[1][0]), xhf[1]); SBAR();
  s16(VB[0], VB1[0]); SBAR();
  MF(acc[1][1][0], xf[9][0], cat(w0[1][1], w1[1][1])); SBAR();
  s16(VB[1], VB1[1]); SBAR();
  MF(acc[1][1][1], xf[9][1], cat(w0[1][1], w1[1][1])); SBAR();
  s16(VB[2], VB1[2]); SBAR();
  MF(ach[1], xhf[1], cat(w0[1][1], w1[1][1])); SBAR();
  s16(VB[3], VB1[3]); SBAR();
  wait_bar<5>(); STAMP(8);
  SBAR();
  MF(acc[1][0][0], cat(w0[2][0], w1[2][0]), xf[10][0]); SBAR();
  w0[0][0] = tr8(wl[0][0] + 32768); SBAR();
  w1[0][0] = tr8(wl[0][0] + 33280); SBAR();
  MF(acc[1][0][1], cat(w0[2][0], w1[2][0]), xf[10][1]); SBAR();
  xhf[0] = lds16(xh[0] + 12288); SBAR();
  wl[2][0] = opaque(wl[0][0] + (L_X - L_W) + ((0 ^ rp) >> 1) * 16384); SBAR();
  MF(acc[1][1][0], xf[10][0], cat(w0[2][1], w1[2][1])); SBAR();
  w0[0][1] = tr8(wl[0][0] + 33792); SBAR();
  issue_w(IC<9>{}, c0); SBAR();
  MF(acc[1][1][1], xf[10][1], cat(w0[2][1], w1[2][1])); SBAR();
  w1[0][1] = tr8(wl[0][0] + 34304); SBAR();
  wl[2][1] = opaque(wl[0][1] + (L_X - L_W) + ((1 ^ rp) >> 1) * 16384); SBAR();
  MF(acc[1][0][0], cat(w0[3][0], w1[3][0]), xf[11][0]); SBAR();
  w0[1][0] = tr8(wl[0][1] + 32768); SBAR();
  wl[2][2] = opaque(wl[0][2] + (L_X - L_W) + ((2 ^ rp) >> 1) * 16384); SBAR();
  MF(acc[1][0][1], cat(w0[3][0], w1[3][0]), xf[11][1]); SBAR();
  w1[1][0] = tr8(wl[0][1] + 33280); SBAR();
  wl[2][3] = opaque(wl[0][3] + (L_X - L_W) + ((3 ^ rp) >> 1) * 16384); SBAR();
  MF(acc[1][1][0], xf[11][0], cat(w0[3][1], w1[3][1])); SBAR();
  xhf[1] = lds16(xh[1] + 12288); SBAR();
  MF(acc[1][1][1], xf[11][1], cat(w0[3][1], w1[3][1])); SBAR();
  w0[1][1] = tr8(wl[0][1] + 33792); SBAR();
  MF(acc[1][0][0], cat(w0[0][0], w1[0][0]), xf[12][0]); SBAR();
  w1[1][1] = tr8(wl[0][1] + 34304); SBAR();
  w0[2][0] = tr8(wl[0][2] + 32768); SBAR();
  MF(acc[1][0][1], cat(w0[0][0], w1[0][0]), xf[12][1]); SBAR();
  w1[2][0] = tr8(wl[0][2] + 33280); SBAR();
  MF(ach[0], cat(w0[0][0], w1[0][0]), xhf[0]); SBAR();
  w0[2][1] = tr8(wl[0][2] + 33792); SBAR();
  issue_w(IC<9>{}, c1); SBAR();
  MF(acc[1][1][0], xf[12][0], cat(w0[0][1], w1[0][1])); SBAR();
  w1[2][1] = tr8(wl[0][2] + 34304); SBAR();
  MF(acc[1][1][1], xf[12][1], cat(w0[0][1], w1[0][1])); SBAR();
  w0[3][0] = tr8(wl[0][3] + 32768); SBAR();
  MF(ach[1], xhf[0], cat(w0[0][1], w1[0][1])); SBAR();
  w1[3][0] = tr8(wl[0][3] + 33280); SBAR();
  MF(acc[1][0][0], cat(w0[1][0], w1[1][0]), xf[13][0]); SBAR();
  w0[3][1] = tr8(wl[0][3] + 33792); SBAR();
  MF(acc[1][0][1], cat(w0[1][0], w1[1][0]), xf[13][1]); SBAR();
  w1[3][1] = tr8(wl[0][3] + 34304); SBAR();
  MF(ach[0], cat(w0[1][0], w1[1][0]), xhf[1]); SBAR();
  MF(acc[1][1][0], xf[13][0], cat(w0[1][1], w1[1][1])); SBAR();
  MF(acc[1][1][1], xf[13][1], cat(w0[1][1], w1[1][1])); SBAR();
  MF(ach[1], xhf[1], cat(w0[1][1], w1[1][1])); SBAR();
  wait_bar<4>(); STAMP(9);
  if (w == 0) st8_sc1(ptag, lane < 4 ? prod_off : OOB, rW, WS_F);
  SBAR();
  MF(acc[1][0][0], cat(w0[2][0], w1[2][0]), xf[14][0]); SBAR();
  w0[0][0] = tr8(wl[2][0] + 0); SBAR();
  w1[0][0] = tr8(wl[2][0] + 512); SBAR();
  MF(acc[1][0][1], cat(w0[2][0], w1[2][0]), xf[14][1]); SBAR();
  w0[0][1] = tr8(wl[2][0] + 1024); SBAR();
  MF(acc[1][1][0], xf[14][0], cat(w0[2][1], w1[2][1])); SBAR();
  w1[0][1] = tr8(wl[2][0] + 1536); SBAR();
  issue_w(IC<10>{}, c0); SBAR();
  MF(acc[1][1][1], xf[14][1], cat(w0[2][1], w1[2][1])); SBAR();
  w0[1][0] = tr8(wl[2][1] + 0); SBAR();
  MF(acc[1][0][0], cat(w0[3][0], w1[3][0]), xf[15][0]); SBAR();
  w1[1][0] = tr8(wl[2][1] + 512); SBAR();
  w0[1][1] = tr8(wl[2][1] + 1024); SBAR();
  MF(acc[1][0][1], cat(w0[3][0], w1[3][0]), xf[15][1]); SBAR();
  w1[1][1] = tr8(wl[2][1] + 1536); SBAR();
  MF(acc[1][1][0], xf[15][0], cat(w0[3][1], w1[3][1])); SBAR();
  w0[2][0] = tr8(wl[2][2] + 0); SBAR();
  MF(acc[1][1][1], xf[15][1], cat(w0[3][1], w1[3][1])); SBAR();
  w1[2][0] = tr8(wl[2][2] + 512); SBAR();
  MF0(acc[2][0][0], cat(w0[0][0], w1[0][0]), xf[0][0]); SBAR();
  w0[2][1] = tr8(wl[2][2] + 1024); SBAR();
  w1[2][1] = tr8(wl[2][2] + 1536); SBAR();
  MF0(acc[2][0][1], cat(w0[0][0], w1[0][0]), xf[0][1]); SBAR();
  w0[3][0] = tr8(wl[2][3] + 0); SBAR();
  issue_w(IC<10>{}, c1); SBAR();
  MF0(acc[2][1][0], cat(w0[0][1], w1[0][1]), xf[0][0]); SBAR();
  w1[3][0] = tr8(wl[2][3] + 512); SBAR();
  cv(KA[0], acc[1][0][0][0], acc[1][0][0][1]); SBAR();
  cv(KA[2], acc[1][0][1][0], acc[1][0][1][1]); SBAR();
  MF0(acc[2][1][1], cat(w0[0][1], w1[0][1]), xf[0][1]); SBAR();
  w0[3][1] = tr8(wl[2][3] + 1024); SBAR();
  cv(KA[1], acc[1][0][0][2], acc[1][0][0][3]); SBAR();
  cv(KA[3], acc[1][0][1][2], acc[1][0][1][3]); SBAR();
  MF(acc[2][0][0], cat(w0[1][0], w1[1][0]), xf[1][0]); SBAR();
  w1[3][1] = tr8(wl[2][3] + 1536); SBAR();
  s16(KA[0], KA[2]); SBAR();
  MF(acc[2][0][1], cat(w0[1][0], w1[1][0]), xf[1][1]); SBAR();
  s16(KA[1], KA[3]); SBAR();
  sel1(ta[0], mtq, __float_as_uint(ach[0][0]), KA[0]); SBAR();
  MF(acc[2][1][0], cat(w0[1][1], w1[1][1]), xf[1][0]); SBAR();
  sel1(ta[1], mtq, __float_as_uint(ach[0][1]), KA[1]); SBAR();
  sel1(ta[2], mtq, __float_as_uint(ach[0][2]), KA[2]); SBAR();
  sel1(ta[3], mtq, __float_as_uint(ach[0][3]), KA[3]); SBAR();
  MF(acc[2][1][1], cat(w0[1][1], w1[1][1]), xf[1][1]); SBAR();
  cv(VA[0], acc[1][1][0][0], acc[1][1][0][1]); SBAR();
  cv(VA[2], acc[1][1][1][0], acc[1][1][1][1]); SBAR();
  cv(VA[1], acc[1][1][0][2], acc[1][1][0][3]); SBAR();
  wait_bar<4>(); STAMP(10);
  SBAR();
  MF(acc[2][0][0], cat(w0[2][0], w1[2][0]), xf[2][0]); SBAR();
  w0[0][0] = tr8(wl[0][0] + 0); SBAR();
  w1[0][0] = tr8(wl[0][0] + 512); SBAR();
  cv(VA[3], acc[1][1][1][2], acc[1][1][1][3]); SBAR();
  MF(acc[2][0][1], cat(w0[2][0], w1[2][0]), xf[2][1]); SBAR();
  w0[0][1] = tr8(wl[0][0] + 1024); SBAR();
  s16(VA[0], VA[2]); SBAR();
  MF(acc[2][1][0], cat(w0[2][1], w1[2][1]), xf[2][0]); SBAR();
  w1[0][1] = tr8(wl[0][0] + 1536); SBAR();
  issue_w(IC<11>{}, c0); SBAR();
  MF(acc[2][1][1], cat(w0[2][1], w1[2][1]), xf[2][1]); SBAR();
  w0[1][0] = tr8(wl[0][1] + 0); SBAR();
  s16(VA[1], VA[3]); SBAR();
  MF(acc[2][0][0], cat(w0[3][0], w1[3][0]), xf[3][0]); SBAR();
  w1[1][0] = tr8(wl[0][1] + 512); SBAR();
  w0[1][1] = tr8(wl[0][1] + 1024); SBAR();
  MF(acc[2][0][1], cat(w0[3][0], w1[3][0]), xf[3][1]); SBAR();
  w1[1][1] = tr8(wl[0][1] + 1536); SBAR();
  s32(VA[0], VA[2]); SBAR();
  MF(acc[2][1][0], cat(w0[3][1], w1[3][1]), xf[3][0]); SBAR();
  w0[2][0] = tr8(wl[0][2] + 0); SBAR();
  s32(VA[1], VA[3]); SBAR();
  MF(acc[2][1][1], cat(w0[3][1], w1[3][1]), xf[3][1]); SBAR();
  w1[2][0] = tr8(wl[0][2] + 512); SBAR();
  VA1[0] = ONE2; pin1(VA1[0]); SBAR();
  VA1[1] = ONE2; pin1(VA1[1]); SBAR();
  MF(acc[2][0][0], cat(w0[0][0], w1[0][0]), xf[4][0]); SBAR();
  w0[2][1] = tr8(wl[0][2] + 1024); SBAR();
  w1[2][1] = tr8(wl[0][2] + 1536); SBAR();
  VA1[2] = ONE2; pin1(VA1[2]); SBAR();
  MF(acc[2][0][1], cat(w0[0][0], w1[0][0]), xf[4][1]); SBAR();
  w0[3][0] = tr8(wl[0][3] + 0); SBAR();
  issue_w(IC<11>{}, c1); SBAR();
  MF(acc[2][1][0], cat(w0[0][1], w1[0][1]), xf[4][0]); SBAR();
  w1[3][0] = tr8(wl[0][3] + 512); SBAR();
  VA1[3] = ONE2; pin1(VA1[3]); SBAR();
  MF(acc[2][1][1], cat(w0[0][1], w1[0][1]), xf[4][1]); SBAR();
  w0[3][1] = tr8(wl[0][3] + 1024); SBAR();
  s16(VA[0], VA1[0]); SBAR();
  MF(acc[2][0][0], cat(w0[1][0], w1[1][0]), xf[5][0]); SBAR();
  w1[3][1] = tr8(wl[0][3] + 1536); SBAR();
  s16(VA[1], VA1[1]); SBAR();
  MF(acc[2][0][1], cat(w0[1][0], w1[1][0]), xf[5][1]); SBAR();
  s16(VA[2], VA1[2]); SBAR();
  MF(acc[2][1][0], cat(w0[1][1], w1[1][1]), xf[5][0]); SBAR();
  s16(VA[3], VA1[3]); SBAR();
  sel1(tc[0], mtq, __float_as_uint(ach[1][0]), VA1[0]); SBAR();
  MF(acc[2][1][1], cat(w0[1][1], w1[1][1]), xf[5][1]); SBAR();
  sel1(tc[1], mtq, __float_as_uint(ach[1][1]), VA1[1]); SBAR();
  sel1(tc[2], mtq, __float_as_uint(ach[1][2]), VA1[2]); SBAR();
  sel1(tc[3], mtq, __float_as_uint(ach[1][3]), VA1[3]); SBAR();
  wait_bar<4>(); STAMP(11);
  SBAR();
  fl = ld8_sc1(cons_off, rW, WS_F); SBAR();
  MF(acc[2][0][0], cat(w0[2][0], w1[2][0]), xf[6][0]); SBAR();
  w0[0][0] = tr8(wl[0][0] + 32768); SBAR();
  w1[0][0] = tr8(wl[0][0] + 33280); SBAR();
  ldwo(IC<0>{}); SBAR();
  MF(acc[2][0][1], cat(w0[2][0], w1[2][0]), xf[6][1]); SBAR();
  w0[0][1] = tr8(wl[0][0] + 33792); SBAR();
  MF(acc[2][1][0], cat(w0[2][1], w1[2][1]), xf[6][0]); SBAR();
  w1[0][1] = tr8(wl[0][0] + 34304); SBAR();
  ldwo(IC<1>{}); SBAR();
  MF(acc[2][1][1], cat(w0[2][1], w1[2][1]), xf[6][1]); SBAR();
  w0[1][0] = tr8(wl[0][1] + 32768); SBAR();
  MF(acc[2][0][0], cat(w0[3][0], w1[3][0]), xf[7][0]); SBAR();
  w1[1][0] = tr8(wl[0][1] + 33280); SBAR();
  w0[1][1] = tr8(wl[0][1] + 33792); SBAR();
  ldwo(IC<2>{}); SBAR();
  MF(acc[2][0][1], cat(w0[3][0], w1[3][0]), xf[7][1]); SBAR();
  w1[1][1] = tr8(wl[0][1] + 34304); SBAR();
  MF(acc[2][1][0], cat(w0[3][1], w1[3][1]), xf[7][0]); SBAR();
  w0[2][0] = tr8(wl[0][2] + 32768); SBAR();
  ldwo(IC<3>{}); SBAR();
  MF(acc[2][1][1], cat(w0[3][1], w1[3][1]), xf[7][1]); SBAR();
  w1[2][0] = tr8(wl[0][2] + 33280); SBAR();
  MF(acc[2][0][0], cat(w0[0][0], w1[0][0]), xf[8][0]); SBAR();
  w0[2][1] = tr8(wl[0][2] + 33792); SBAR();
  w1[2][1] = tr8(wl[0][2] + 34304); SBAR();
  MF(acc[2][0][1], cat(w0[0][0], w1[0][0]), xf[8][1]); SBAR();
  w0[3][0] = tr8(wl[0][3] + 32768); SBAR();
  MF(acc[2][1][0], cat(w0[0][1], w1[0][1]), xf[8][0]); SBAR();
  w1[3][0] = tr8(wl[0][3] + 33280); SBAR();
  MF(acc[2][1][1], cat(w0[0][1], w1[0][1]), xf[8][1]); SBAR();
  w0[3][1] = tr8(wl[0][3] + 33792); SBAR();
  MF(acc[2][0][0], cat(w0[1][0], w1[1][0]), xf[9][0]); SBAR();
  w1[3][1] = tr8(wl[0][3] + 34304); SBAR();
  MF(acc[2][0][1], cat(w0[1][0], w1[1][0]), xf[9][1]); SBAR();
  MF(acc[2][1][0], cat(w0[1][1], w1[1][1]), xf[9][0]); SBAR();
  MF(acc[2][1][1], cat(w0[1][1], w1[1][1]), xf[9][1]); SBAR();
  *(LAS u32x4*)ex = v4(ta); SBAR();
  *(LAS u32x4*)(ex + 1024) = v4(tc); SBAR();
  wait2<4>(fl); SBAR();
  { const u64 need = (1ull << min(tb, 4)) - 1; if (__builtin_expect((flags_off(fl, ctag) & need) != 0, 0)) poll(cons_off + WS_F, rW, ctag, need); } SBAR();
  ldt(IC<0>{}); SBAR();
  wait_bar<8>(); STAMP(12);
  SBAR();
  MF(acc[2][0][0], cat(w0[2][0], w1[2][0]), xf[10][0]); SBAR();
  w0[0][0] = tr8(wl[1][0] + 0); SBAR();
  w1[0][0] = tr8(wl[1][0] + 512); SBAR();
  e0 = lds16(ex + (tq ? -2048 : 2048)); SBAR();
  MF(acc[2][0][1], cat(w0[2][0], w1[2][0]), xf[10][1]); SBAR();
  w0[0][1] = tr8(wl[1][0] + 1024); SBAR();
  ldwo(IC<4>{}); SBAR();
  e1 = lds16(ex + (tq ? -1024 : 3072)); SBAR();
  MF(acc[2][1][0], cat(w0[2][1], w1[2][1]), xf[10][0]); SBAR();
  w1[0][1] = tr8(wl[1][0] + 1536); SBAR();
  MF(acc[2][1][1], cat(w0[2][1], w1[2][1]), xf[10][1]); SBAR();
  w0[1][0] = tr8(wl[1][1] + 0); SBAR();
  f0 = ach[0] + __builtin_bit_cast(f32x4, e0); pinf(f0); SBAR();
  MF(acc[2][0][0], cat(w0[3][0], w1[3][0]), xf[11][0]); SBAR();
  w1[1][0] = tr8(wl[1][1] + 512); SBAR();
  w0[1][1] = tr8(wl[1][1] + 1024); SBAR();
  MF(acc[2][0][1], cat(w0[3][0], w1[3][0]), xf[11][1]); SBAR();
  w1[1][1] = tr8(wl[1][1] + 1536); SBAR();
  ldwo(IC<5>{}); SBAR();
  MF(acc[2][1][0], cat(w0[3][1], w1[3][1]), xf[11][0]); SBAR();
  w0[2][0] = tr8(wl[1][2] + 0); SBAR();
  f1 = ach[1] + __builtin_bit_cast(f32x4, e1); pinf(f1); SBAR();
  MF(acc[2][1][1], cat(w0[3][1], w1[3][1]), xf[11][1]); SBAR();
  w1[2][0] = tr8(wl[1][2] + 512); SBAR();
  cv(ta[0], f0[0], f0[1]); SBAR();
  cv(ta[1], f0[2], f0[3]); SBAR();
  MF(acc[2][0][0], cat(w0[0][0], w1[0][0]), xf[12][0]); SBAR();
  w0[2][1] = tr8(wl[1][2] + 1024); SBAR();
  w1[2][1] = tr8(wl[1][2] + 1536); SBAR();
  cv(tc[0], f1[0], f1[1]); SBAR();
  MF(acc[2][0][1], cat(w0[0][0], w1[0][0]), xf[12][1]); SBAR();
  w0[3][0] = tr8(wl[1][3] + 0); SBAR();
  ldwo(IC<6>{}); SBAR();
  cv(tc[1], f1[2], f1[3]); SBAR();
  MF(acc[2][1][0], cat(w0[0][1], w1[0][1]), xf[12][0]); SBAR();
  w1[3][0] = tr8(wl[1][3] + 512); SBAR();
  MF(acc[2][1][1], cat(w0[0][1], w1[0][1]), xf[12][1]); SBAR();
  w0[3][1] = tr8(wl[1][3] + 1024); SBAR();
  MF(acc[2][0][0], cat(w0[1][0], w1[1][0]), xf[13][0]); SBAR();
  w1[3][1] = tr8(wl[1][3] + 1536); SBAR();
  MF(acc[2][0][1], cat(w0[1][0], w1[1][0]), xf[13][1]); SBAR();
  ldwo(IC<7>{}); SBAR();
  MF(acc[2][1][0], cat(w0[1][1], w1[1][1]), xf[13][0]); SBAR();
  ta[2] = ta[0]; s16(ta[0], ta[2]); SBAR();
  MF(acc[2][1][1], cat(w0[1][1], w1[1][1]), xf[13][1]); SBAR();
  ta[3] = ta[1]; s16(ta[1], ta[3]); SBAR();
  wait_bar<63>();
  const LAS char* ml = opaque(sm + L_M + lane * 16);
  f32x16 sa, sb, oA, oB;
  u32 pa[4], pb[4], old;
  u32x4 QA, QB;
  const f32x16 zero = {};
  auto mq = [&](int i) AI { return *(const LAS f32x4*)(ml + i * 1024); };
  auto mask = [&](int m) AI { return cat16(mq(4 * m), mq(4 * m + 1), mq(4 * m + 2), mq(4 * m + 3)); };
  auto maskp = [&]() AI { const f32x4 a = mq(12), c = mq(13); return cat16(a, c, a, c); };
  constexpr float QS = 0.25f * 1.4426950408889634f;
  const int tg = w & 3, kh = w >> 2;
  auto finish_a = [&]() AI {
    pin16(oA);
    if (__builtin_expect(rows_bad(oA), 0)) {
      float m = tile_max<0>(v4(KA), QA, mask(2), -__builtin_inff());
      if (tile >= 1) m = tile_max<8>(v4(AK), QA, maskp(), m);
      m = -xhalf_max(m);
      oA = zero;
      tile_full(v4(KA), QA, v4(VA), v4(VA1), addc(mask(2), m), oA);
      if (tile >= 1) tile_upper(v4(AK), QA, v4(AV), addc(maskp(), m), oA);
    }
    st16_sc1(pk8<0>(oA, __builtin_amdgcn_rcpf(oA[8])), vL, rW, oa_own);
    st16_sc1(u32x4{PZN, PZN, PZN, PZN}, vL, rW, ob_own);
    st4_sc1(zb, lane < 32 ? (u32)(lane * 4) : OOB, rW, hg == (tb & 7) ? WS_Z + (u32)(tb * 1024 + hh * 256 + tq * 128) : OOB);
  };
  auto arrive = [&]() AI { old = 0; if (lane == 0) old = __hip_atomic_fetch_add((LAS u32*)(sm + L_CNT), 1u, __ATOMIC_RELAXED, __HIP_MEMORY_SCOPE_WORKGROUP); };
  auto signal_a = [&]() AI { if (__builtin_amdgcn_readfirstlane(old) == 7) st8_sc1(ptag, (lane >> 3) == 1 ? prod_off : OOB, rW, WS_F); };
  MF(acc[2][0][0], cat(w0[2][0], w1[2][0]), xf[14][0]); SBAR();
  ldm(sa, 0, ml + 8192); SBAR();
  tc[2] = tc[0]; s16(tc[0], tc[2]); SBAR();
  MF(acc[2][0][1], cat(w0[2][0], w1[2][0]), xf[14][1]); SBAR();
  ldm(sa, 1, ml + 9216); SBAR();
  tc[3] = tc[1]; s16(tc[1], tc[3]); SBAR();
  MF(acc[2][1][0], cat(w0[2][1], w1[2][1]), xf[14][0]); SBAR();
  ldm(sa, 2, ml + 10240); SBAR();
  s32(tc[0], tc[2]); SBAR();
  MF(acc[2][1][1], cat(w0[2][1], w1[2][1]), xf[14][1]); SBAR();
  ldm(sa, 3, ml + 11264); SBAR();
  s32(tc[1], tc[3]); SBAR();
  MF(acc[2][0][0], cat(w0[3][0], w1[3][0]), xf[15][0]); SBAR();
  ldm(sb, 2, ml + 12288); SBAR();
  sel1(AK[0], mtq, e0[0], ta[0]); SBAR();
  sel1(AK[1], mtq, e0[1], ta[1]); SBAR();
  MF(acc[2][0][1], cat(w0[3][0], w1[3][0]), xf[15][1]); SBAR();
  ldm(sb, 3, ml + 13312); SBAR();
  sel1(AK[2], mtq, e0[2], ta[2]); SBAR();
  sel1(AK[3], mtq, e0[3], ta[3]); SBAR();
  MF(acc[2][1][0], cat(w0[3][1], w1[3][1]), xf[15][0]); SBAR();
  ldm(sb, 0, ml + 12288); SBAR();
  one_h = tb > 0 ? ONE2 : 0u; pin1(one_h); SBAR();
  sel1(tc[0], 0xFFFF0000FFFF0000ull, one_h, tc[0]); SBAR();
  MF(acc[2][1][1], cat(w0[3][1], w1[3][1]), xf[15][1]); SBAR();
  ldm(sb, 1, ml + 13312); SBAR();
  sel1(tc[1], 0xFFFF0000FFFF0000ull, one_h, tc[1]); SBAR();
  sel1(tc[2], 0xFFFF0000FFFF0000ull, one_h, tc[2]); SBAR();
  sel1(tc[3], 0xFFFF0000FFFF0000ull, one_h, tc[3]); SBAR();
  sel1(AV[0], mtq, e1[0], tc[0]); SBAR();
  sel1(AV[1], mtq, e1[1], tc[1]); SBAR();
  sel1(AV[2], mtq, e1[2], tc[2]); SBAR();
  sel1(AV[3], mtq, e1[3], tc[3]); SBAR();
  ldt(IC<1>{}); SBAR();
  asm volatile("s_nop 3" ::: "memory"); SBAR();
  STAMP(13); __builtin_amdgcn_s_setprio(3); SBAR();
  QA = frag_kq(pk4(acc[2][0][0], QS), pk4(acc[2][0][1], QS)); pin4(QA); SBAR();
  asm volatile("s_nop 1" ::: "memory"); SBAR();
  MF(sa, v4(KA), QA); SBAR();
  QB = frag_kq(pk4(acc[2][1][0], QS), pk4(acc[2][1][1], QS)); pin4(QB); SBAR();
  STAMP(14); SBAR();
  ex1(sa, 0); SBAR();
  ex1(sa, 1); SBAR();
  MF(sb, v4(AK), QA); SBAR();
  ex1(sa, 2); SBAR();
  ex1(sa, 3); SBAR();
  ex1(sa, 4); SBAR();
  ex1(sa, 5); SBAR();
  ex1(sa, 6); SBAR();
  ex1(sa, 7); SBAR();
  cv(pa[0], sa[0], sa[1]); SBAR();
  cv(pa[1], sa[2], sa[3]); SBAR();
  cv(pa[2], sa[4], sa[5]); SBAR();
  cv(pa[3], sa[6], sa[7]); SBAR();
  ex1(sa, 8); SBAR();
  ex1(sa, 9); SBAR();
  MF0(oA, v4(VA), v4(pa)); SBAR();
  ex1(sa, 10); SBAR();
  ex1(sa, 11); SBAR();
  ex1(sa, 12); SBAR();
  ex1(sa, 13); SBAR();
  ex1(sa, 14); SBAR();
  ex1(sa, 15); SBAR();
  cv(pb[0], sa[8], sa[9]); SBAR();
  cv(pb[1], sa[10], sa[11]); SBAR();
  cv(pb[2], sa[12], sa[13]); SBAR();
  cv(pb[3], sa[14], sa[15]); SBAR();
  ldm(sa, 0, ml + 0); SBAR();
  ldm(sa, 1, ml + 1024); SBAR();
  ldm(sa, 2, ml + 2048); SBAR();
  ldm(sa, 3, ml + 3072); SBAR();
  ldt(IC<2>{}); SBAR();
  ldt(IC<3>{}); SBAR();
  STAMP(15); SBAR();
  pin16(sb); SBAR();
  ex1(sb, 8); SBAR();
  ex1(sb, 9); SBAR();
  MF(oA, v4(VA1), v4(pb)); SBAR();
  ex1(sb, 10); SBAR();
  ex1(sb, 11); SBAR();
  ex1(sb, 12); SBAR();
  ex1(sb, 13); SBAR();
  ex1(sb, 14); SBAR();
  ex1(sb, 15); SBAR();
  MF(sa, v4(KB), QB); SBAR();
  cv(pb[0], sb[8], sb[9]); SBAR();
  cv(pb[1], sb[10], sb[11]); SBAR();
  cv(pb[2], sb[12], sb[13]); SBAR();
  cv(pb[3], sb[14], sb[15]); SBAR();
  asm volatile("s_nop 1" ::: "memory"); SBAR();
  MF(oA, v4(AV), v4(pb)); SBAR();
  asm volatile("s_nop 3" ::: "memory"); SBAR();
  STAMP(16); SBAR();
  ex1(sa, 0); SBAR();
  ex1(sa, 1); SBAR();
  wait3<13>(kf[0], v0[0], v1[0]); SBAR();
  MF0(sb, kf[0], QB); SBAR();
  ex1(sa, 2); SBAR();
  ex1(sa, 3); SBAR();
  ex1(sa, 4); SBAR();
  ex1(sa, 5); SBAR();
  ex1(sa, 6); SBAR();
  ex1(sa, 7); SBAR();
  cv(pa[0], sa[0], sa[1]); SBAR();
  cv(pa[1], sa[2], sa[3]); SBAR();
  cv(pa[2], sa[4], sa[5]); SBAR();
  cv(pa[3], sa[6], sa[7]); SBAR();
  ex1(sa, 8); SBAR();
  ex1(sa, 9); SBAR();
  MF0(oB, v4(VB), v4(pa)); SBAR();
  ex1(sa, 10); SBAR();
  ex1(sa, 11); SBAR();
  ex1(sa, 12); SBAR();
  ex1(sa, 13); SBAR();
  ex1(sa, 14); SBAR();
  ex1(sa, 15); SBAR();
  cv(pb[0], sa[8], sa[9]); SBAR();
  cv(pb[1], sa[10], sa[11]); SBAR();
  cv(pb[2], sa[12], sa[13]); SBAR();
  cv(pb[3], sa[14], sa[15]); SBAR();
  finish_a(); SBAR();
  ldt(IC<4>{}); SBAR();
  __builtin_amdgcn_s_setprio(2); SBAR();
  STAMP(17); SBAR();
  ex1(sb, 0); SBAR();
  ex1(sb, 1); SBAR();
  MF(oB, v4(VB1), v4(pb)); SBAR();
  wait3<12>(kf[1], v0[1], v1[1]); SBAR();
  MF0(sa, kf[1], QB); SBAR();
  ex1(sb, 2); SBAR();
  ex1(sb, 3); SBAR();
  ex1(sb, 4); SBAR();
  ex1(sb, 5); SBAR();
  ex1(sb, 6); SBAR();
  ex1(sb, 7); SBAR();
  cv(pa[0], sb[0], sb[1]); SBAR();
  cv(pa[1], sb[2], sb[3]); SBAR();
  cv(pa[2], sb[4], sb[5]); SBAR();
  cv(pa[3], sb[6], sb[7]); SBAR();
  ex1(sb, 8); SBAR();
  ex1(sb, 9); SBAR();
  MF(oB, v0[0], v4(pa)); SBAR();
  ex1(sb, 10); SBAR();
  ex1(sb, 11); SBAR();
  ex1(sb, 12); SBAR();
  ex1(sb, 13); SBAR();
  ex1(sb, 14); SBAR();
  ex1(sb, 15); SBAR();
  cv(pb[0], sb[8], sb[9]); SBAR();
  cv(pb[1], sb[10], sb[11]); SBAR();
  cv(pb[2], sb[12], sb[13]); SBAR();
  cv(pb[3], sb[14], sb[15]); SBAR();
  ldt(IC<5>{}); SBAR();
  STAMP(18); SBAR();
  ex1(sa, 0); SBAR();
  ex1(sa, 1); SBAR();
  MF(oB, v1[0], v4(pb)); SBAR();
  wait3<12>(kf[2], v0[2], v1[2]); SBAR();
  MF0(sb, kf[2], QB); SBAR();
  ex1(sa, 2); SBAR();
  ex1(sa, 3); SBAR();
  ex1(sa, 4); SBAR();
  ex1(sa, 5); SBAR();
  ex1(sa, 6); SBAR();
  ex1(sa, 7); SBAR();
  cv(pa[0], sa[0], sa[1]); SBAR();
  cv(pa[1], sa[2], sa[3]); SBAR();
  cv(pa[2], sa[4], sa[5]); SBAR();
  cv(pa[3], sa[6], sa[7]); SBAR();
  ex1(sa, 8); SBAR();
  ex1(sa, 9); SBAR();
  MF(oB, v0[1], v4(pa)); SBAR();
  ex1(sa, 10); SBAR();
  ex1(sa, 11); SBAR();
  ex1(sa, 12); SBAR();
  ex1(sa, 13); SBAR();
  ex1(sa, 14); SBAR();
  ex1(sa, 15); SBAR();
  cv(pb[0], sa[8], sa[9]); SBAR();
  cv(pb[1], sa[10], sa[11]); SBAR();
  cv(pb[2], sa[12], sa[13]); SBAR();
  cv(pb[3], sa[14], sa[15]); SBAR();
  ldt(IC<6>{}); SBAR();
  STAMP(19); SBAR();
  ex1(sb, 0); SBAR();
  ex1(sb, 1); SBAR();
  MF(oB, v1[1], v4(pb)); SBAR();
  wait3<12>(kf[3], v0[3], v1[3]); SBAR();
  MF0(sa, kf[3], QB); SBAR();
  ex1(sb, 2); SBAR();
  ex1(sb, 3); SBAR();
  ex1(sb, 4); SBAR();
  ex1(sb, 5); SBAR();
  ex1(sb, 6); SBAR();
  ex1(sb, 7); SBAR();
  cv(pa[0], sb[0], sb[1]); SBAR();
  cv(pa[1], sb[2], sb[3]); SBAR();
  cv(pa[2], sb[4], sb[5]); SBAR();
  cv(pa[3], sb[6], sb[7]); SBAR();
  ex1(sb, 8); SBAR();
  ex1(sb, 9); SBAR();
  MF(oB, v0[2], v4(pa)); SBAR();
  ex1(sb, 10); SBAR();
  ex1(sb, 11); SBAR();
  ex1(sb, 12); SBAR();
  ex1(sb, 13); SBAR();
  ex1(sb, 14); SBAR();
  ex1(sb, 15); SBAR();
  cv(pb[0], sb[8], sb[9]); SBAR();
  cv(pb[1], sb[10], sb[11]); SBAR();
  cv(pb[2], sb[12], sb[13]); SBAR();
  cv(pb[3], sb[14], sb[15]); SBAR();
  ldt(IC<7>{}); SBAR();
  __builtin_amdgcn_s_setprio(1); SBAR();
  STAMP(20); SBAR();
  ex1(sa, 0); SBAR();
  ex1(sa, 1); SBAR();
  MF(oB, v1[2], v4(pb)); SBAR();
  wait3<9>(kf[4], v0[4], v1[4]); SBAR();
  arrive(); SBAR();
  MF0(sb, kf[4], QB); SBAR();
  ex1(sa, 2); SBAR();
  ex1(sa, 3); SBAR();
  ex1(sa, 4); SBAR();
  ex1(sa, 5); SBAR();
  ex1(sa, 6); SBAR();
  ex1(sa, 7); SBAR();
  cv(pa[0], sa[0], sa[1]); SBAR();
  cv(pa[1], sa[2], sa[3]); SBAR();
  cv(pa[2], sa[4], sa[5]); SBAR();
  cv(pa[3], sa[6], sa[7]); SBAR();
  ex1(sa, 8); SBAR();
  ex1(sa, 9); SBAR();
  MF(oB, v0[3], v4(pa)); SBAR();
  ex1(sa, 10); SBAR();
  ex1(sa, 11); SBAR();
  ex1(sa, 12); SBAR();
  ex1(sa, 13); SBAR();
  ex1(sa, 14); SBAR();
  ex1(sa, 15); SBAR();
  cv(pb[0], sa[8], sa[9]); SBAR();
  cv(pb[1], sa[10], sa[11]); SBAR();
  cv(pb[2], sa[12], sa[13]); SBAR();
  cv(pb[3], sa[14], sa[15]); SBAR();
  signal_a(); SBAR();
  ldwo(IC<8>{}); SBAR();
  ldwo(IC<9>{}); SBAR();
  ldwo(IC<10>{}); SBAR();
  STAMP(21); SBAR();
  ex1(sb, 0); SBAR();
  ex1(sb, 1); SBAR();
  MF(oB, v1[3], v4(pb)); SBAR();
  wait3<9>(kf[5], v0[5], v1[5]); SBAR();
  MF0(sa, kf[5], QB); SBAR();
  ex1(sb, 2); SBAR();
  ex1(sb, 3); SBAR();
  ex1(sb, 4); SBAR();
  ex1(sb, 5); SBAR();
  ex1(sb, 6); SBAR();
  ex1(sb, 7); SBAR();
  cv(pa[0], sb[0], sb[1]); SBAR();
  cv(pa[1], sb[2], sb[3]); SBAR();
  cv(pa[2], sb[4], sb[5]); SBAR();
  cv(pa[3], sb[6], sb[7]); SBAR();
  ex1(sb, 8); SBAR();
  ex1(sb, 9); SBAR();
  MF(oB, v0[4], v4(pa)); SBAR();
  ex1(sb, 10); SBAR();
  ex1(sb, 11); SBAR();
  ex1(sb, 12); SBAR();
  ex1(sb, 13); SBAR();
  ex1(sb, 14); SBAR();
  ex1(sb, 15); SBAR();
  cv(pb[0], sb[8], sb[9]); SBAR();
  cv(pb[1], sb[10], sb[11]); SBAR();
  cv(pb[2], sb[12], sb[13]); SBAR();
  cv(pb[3], sb[14], sb[15]); SBAR();
  ldwo(IC<11>{}); SBAR();
  ldwo(IC<12>{}); SBAR();
  ldwo(IC<13>{}); SBAR();
  STAMP(22); SBAR();
  ex1(sa, 0); SBAR();
  ex1(sa, 1); SBAR();
  MF(oB, v1[4], v4(pb)); SBAR();
  wait3<9>(kf[6], v0[6], v1[6]); SBAR();
  MF0(sb, kf[6], QB); SBAR();
  ex1(sa, 2); SBAR();
  ex1(sa, 3); SBAR();
  ex1(sa, 4); SBAR();
  ex1(sa, 5); SBAR();
  ex1(sa, 6); SBAR();
  ex1(sa, 7); SBAR();
  cv(pa[0], sa[0], sa[1]); SBAR();
  cv(pa[1], sa[2], sa[3]); SBAR();
  cv(pa[2], sa[4], sa[5]); SBAR();
  cv(pa[3], sa[6], sa[7]); SBAR();
  ex1(sa, 8); SBAR();
  ex1(sa, 9); SBAR();
  MF(oB, v0[5], v4(pa)); SBAR();
  ex1(sa, 10); SBAR();
  ex1(sa, 11); SBAR();
  ex1(sa, 12); SBAR();
  ex1(sa, 13); SBAR();
  ex1(sa, 14); SBAR();
  ex1(sa, 15); SBAR();
  cv(pb[0], sa[8], sa[9]); SBAR();
  cv(pb[1], sa[10], sa[11]); SBAR();
  cv(pb[2], sa[12], sa[13]); SBAR();
  cv(pb[3], sa[14], sa[15]); SBAR();
  ldm(sa, 0, ml + 4096); SBAR();
  ldm(sa, 1, ml + 5120); SBAR();
  ldm(sa, 2, ml + 6144); SBAR();
  ldm(sa, 3, ml + 7168); SBAR();
  ldwo(IC<14>{}); SBAR();
  ldwo(IC<15>{}); SBAR();
  __builtin_amdgcn_s_setprio(0); SBAR();
  STAMP(23); SBAR();
  ex1(sb, 0); SBAR();
  ex1(sb, 1); SBAR();
  MF(oB, v1[5], v4(pb)); SBAR();
  wait3<8>(kf[7], v0[7], v1[7]); SBAR();
  MF(sa, kf[7], QB); SBAR();
  ex1(sb, 2); SBAR();
  ex1(sb, 3); SBAR();
  ex1(sb, 4); SBAR();
  ex1(sb, 5); SBAR();
  ex1(sb, 6); SBAR();
  ex1(sb, 7); SBAR();
  cv(pa[0], sb[0], sb[1]); SBAR();
  cv(pa[1], sb[2], sb[3]); SBAR();
  cv(pa[2], sb[4], sb[5]); SBAR();
  cv(pa[3], sb[6], sb[7]); SBAR();
  ex1(sb, 8); SBAR();
  ex1(sb, 9); SBAR();
  MF(oB, v0[6], v4(pa)); SBAR();
  ex1(sb, 10); SBAR();
  ex1(sb, 11); SBAR();
  ex1(sb, 12); SBAR();
  ex1(sb, 13); SBAR();
  ex1(sb, 14); SBAR();
  ex1(sb, 15); SBAR();
  cv(pb[0], sb[8], sb[9]); SBAR();
  cv(pb[1], sb[10], sb[11]); SBAR();
  cv(pb[2], sb[12], sb[13]); SBAR();
  cv(pb[3], sb[14], sb[15]); SBAR();
  STAMP(24); SBAR();
  ex1(sa, 0); SBAR();
  ex1(sa, 1); SBAR();
  MF(oB, v1[6], v4(pb)); SBAR();
  ex1(sa, 2); SBAR();
  ex1(sa, 3); SBAR();
  ex1(sa, 4); SBAR();
  ex1(sa, 5); SBAR();
  ex1(sa, 6); SBAR();
  ex1(sa, 7); SBAR();
  cv(pa[0], sa[0], sa[1]); SBAR();
  cv(pa[1], sa[2], sa[3]); SBAR();
  cv(pa[2], sa[4], sa[5]); SBAR();
  cv(pa[3], sa[6], sa[7]); SBAR();
  ex1(sa, 8); SBAR();
  ex1(sa, 9); SBAR();
  MF(oB, v0[7], v4(pa)); SBAR();
  ex1(sa, 10); SBAR();
  ex1(sa, 11); SBAR();
  ex1(sa, 12); SBAR();
  ex1(sa, 13); SBAR();
  ex1(sa, 14); SBAR();
  ex1(sa, 15); SBAR();
  cv(pb[0], sa[8], sa[9]); SBAR();
  cv(pb[1], sa[10], sa[11]); SBAR();
  cv(pb[2], sa[12], sa[13]); SBAR();
  cv(pb[3], sa[14], sa[15]); SBAR();
  asm volatile("s_nop 1" ::: "memory"); SBAR();
  MF(oB, v1[7], v4(pb)); SBAR();
  asm volatile("s_nop 7\n\ts_nop 3" ::: "memory"); SBAR();
  pin16(oB);
  if (__builtin_expect(rows_bad(oB), 0)) {
    float m = tile_max<0>(v4(KB), QB, mask(0), -__builtin_inff());
    sfor<0, 8>([&](auto Dd) AI { constexpr int d = Dd.v; if (tile - d - 1 >= 0) m = tile_max<0>(kf[d], QB, d == 7 ? mask(1) : zero, m); });
    m = -xhalf_max(m);
    oB = zero;
    tile_full(v4(KB), QB, v4(VB), v4(VB1), addc(mask(0), m), oB);
    const f32x16 cm = addc(zero, m);
    sfor<0, 8>([&](auto Dd) AI { constexpr int d = Dd.v; if (tile - d - 1 >= 0) tile_full(kf[d], QB, v0[d], v1[d], d == 7 ? addc(mask(1), m) : cm, oB); });
  }
  STAMP(27);
  fl = ld8_sc1(cons_off, rW, WS_F);
  st16_sc1(nopz(pk8<0>(oB, __builtin_amdgcn_rcpf(oB[8]))), vL, rW, ob_own);
  wait_bar<2>();
  if (w == 0) st8_sc1(ptag, (lane >> 3) == 2 ? prod_off : OOB, rW, WS_F);
  STAMP(28);
  const u32 vo = (u32)(h * 1024 + (g & 1) * 512 + ((tg & 1) * 16 + i16) * 16);
  const u32 so = WS_O + (u32)((2 * to + (tg >> 1)) * 64 + 16 * kh) * 1024;
  u32x4 oa[8], of[8];
  const LAS char* ol[4];
#pragma unroll
  for (int t = 0; t < 4; ++t) ol[t] = sm + L_W + kh * 32768 + (16 * h + 4 * (g & 1) + (i16 >> 2)) * 128 + ((t ^ (2 * (g & 1) + (i16 >> 3))) * 32) + (i16 & 3) * 8;
  u32x4 wf[8][4];
  auto rdo = [&](auto M) AI { constexpr int m = M.v;
    if constexpr (m == 8) {
#pragma unroll
      for (int t = 0; t < 4; ++t) ol[t] = opaque(ol[t] + 65536);
    }
#pragma unroll
    for (int t = 0; t < 4; ++t) wf[m & 7][t] = ldtr(ol[t] + (m & 7) * 4096, 1024);
    SBAR();
  };
  rdo(c0); rdo(c1);
  wait2<1>(fl);
  const u64 off = flags_off(fl, ctag), needb = 0xF0000ull << (4 * kh);
  if (__builtin_expect((off & 0xFF00ull) != 0, 0)) poll(cons_off + WS_F, rW, ctag, 0xFF00ull);
  STAMP(29);
  u32 zl = ld4_sc1((u32)(g * 256 + (tg * 16 + i16) * 4), rW, WS_Z + (u32)to * 1024);
  sfor<0, 8>([&](auto M) AI { constexpr int m = M.v; oa[m] = ld16_sc1(vo, rW, so + m * 2048); SBAR(); if constexpr (m < 6) rdo(IC<m + 2>{}); });
  auto ldo = [&]() AI { sfor<0, 8>([&](auto M) AI { constexpr int m = M.v; of[m] = ld16_sc1(vo, rW, so + 32768 + m * 2048); }); };
  const LAS char* rw = opaque(sm + L_RED + w * 2048 + lane * 16);
  const LAS char* rr_ = opaque(sm + L_RED + (w ^ 4) * 2048 + lane * 16);
  u32 vout = (u32)(((to * 64 + tg * 16 + i16) * 512 + cg * 64 + 32 * kh + 16 * (g & 1) + 8 * h) * 2);
  pin1(vout);
  f32x4 oc[4];
  const u32 late = (u32)((off & needb) >> 16);
  sfor<0, 8>([&](auto M) AI {
    constexpr int m = M.v;
    wait_a_load_b<m>(oa[m], of[m], late, vo, rW, so + 32768 + m * 2048);
    SBAR();
#pragma unroll
    for (int t = 0; t < 4; ++t) oc[t] = mf16(wf[m][t], oa[m], m ? oc[t] : f32x4{});
    SBAR();
    rdo(IC<m + 8>{});
  });
  STAMP(30);
  if (late) { poll(cons_off + WS_F, rW, ctag, needb); ldo(); }
  STAMP(31);
  float gate;
  {
    pin1(zl);
    u32 a = zl, c = a;
    s16(a, c);
    a = __float_as_uint(__uint_as_float(a) + __uint_as_float(c)); c = a;
    s32(a, c);
    gate = __builtin_amdgcn_rcpf(1.f + __builtin_amdgcn_exp2f(-1.4426950408889634f * (__uint_as_float(a) + __uint_as_float(c))));
  }
#pragma unroll
  for (int t = 0; t < 4; ++t) { oc[t] *= gate; pinf(oc[t]); }
  gate = 1.f - gate;
  f32x4 od[4];
  {
    u32 mx = 0;
    sfor<0, 8>([&](auto M) AI {
      constexpr int m = M.v;
      wait1<7 - m>(of[m]);
#pragma unroll
      for (int t = 0; t < 4; ++t) od[t] = mf16(wf[m][t], of[m], m ? od[t] : f32x4{});
      mx = umax4(mx, of[m]);
      SBAR();
    });
    if (__builtin_expect(__builtin_amdgcn_ballot_w64(mx == PZN) != 0, 0)) {
#pragma unroll 1
      for (int spin = 0; spin < (1 << 16); ++spin) {
        ldo(); waitv<0>();
        u32 m2 = 0;
        sfor<0, 8>([&](auto M) AI { pin4(of[M.v]); m2 = umax4(m2, of[M.v]); });
        if (__builtin_amdgcn_ballot_w64(m2 == PZN) == 0) break;
      }
      sfor<0, 8>([&](auto M) AI {
        constexpr int m = M.v;
#pragma unroll
        for (int t = 0; t < 4; ++t) od[t] = mf16(wf[m][t], of[m], m ? od[t] : f32x4{});
      });
    }
  }
  STAMP(32);
  f32x4 k0, k1;
  if (kh) { *(LAS f32x4*)rw = od[0] * gate + oc[0]; *(LAS f32x4*)(rw + 1024) = od[1] * gate + oc[1]; k0 = od[2] * gate + oc[2]; k1 = od[3] * gate + oc[3]; }
  else { *(LAS f32x4*)rw = od[2] * gate + oc[2]; *(LAS f32x4*)(rw + 1024) = od[3] * gate + oc[3]; k0 = od[0] * gate + oc[0]; k1 = od[1] * gate + oc[1]; }
  asm volatile("s_waitcnt lgkmcnt(0)\n\ts_barrier" ::: "memory");
  st16(frag_kq(pk4(k0 + *(const LAS f32x4*)rr_), pk4(k1 + *(const LAS f32x4*)(rr_ + 1024))), vout, mkrs(out, 2048 * 512 * 2), 0);
  if (w == 0) st8(ctag, cons_off, rW, WS_E);
}

extern "C" void kernel_launch(void* const* d_in, const int* in_sizes, int n_in, void* d_out, int out_size, void* d_ws, size_t ws_size,
                              hipStream_t stream) {
  gated_mix_kernel<<<256, 512, 0, stream>>>((const u16*)d_in[0], (const u16*)d_in[1], (const u16*)d_in[2], (const u16*)d_in[4],
                                            (const u16*)d_in[5], (const u16*)d_in[6], (u16*)d_out, (char*)d_ws);
}
```
